```python
import torch

def _softmax(x, axis=-1):
    return torch.softmax(x, dim=axis)

def _gelu(x):
    return torch.nn.functional.gelu(x, approximate='tanh')

def _silu(x):
    return torch.nn.functional.silu(x)

def _sigmoid(x):
    return torch.sigmoid(x)

def _layernorm(x, axis=-1, eps=1e-05):
    m = x.mean(dim=axis, keepdim=True)
    v = x.var(dim=axis, keepdim=True, unbiased=False)
    return (x - m) / torch.sqrt(v + eps)

def _scatter_add(tgt, idx, src, axis=0):
    return tgt.clone().index_add_(axis, idx, src)

def _segment_sum(src, seg, n):
    out = torch.zeros((n,) + src.shape[1:], dtype=src.dtype)
    return out.index_add_(0, seg, src)

def _pad(x, pw):
    flat = tuple((v for pair in reversed(pw) for v in pair))
    return torch.nn.functional.pad(x, flat)
kernel_name = 'interleave_layers__memory_aug_recurrent_mem_tr__mixer_channel_mlp__prompt'

def setup_inputs():

    _w = lambda a: (lambda t: t.to(torch.bfloat16) if t.is_floating_point() else t)(torch.as_tensor(a))

    def _A_setup():
        B, S, Dm = (1, 2048, 1024)
        H, Dh = (32, 128)
        M, seg = (8, 32)
        seg = min(seg, S)
        S = S // seg * seg
        return {'x': _w((lambda *s: torch.randn(*s, dtype=torch.bfloat16) * 0.05)(B, S, Dm)), 'mem0': _w((lambda *s: torch.randn(*s, dtype=torch.bfloat16) * 0.8)(B, M, Dm)), 'Wq': _w((lambda *s: torch.randn(*s, dtype=torch.bfloat16) * 1.6)(Dm, H, Dh) * Dm ** (-0.5)), 'Wk': _w((lambda *s: torch.randn(*s, dtype=torch.bfloat16) * 1.6)(Dm, H, Dh) * Dm ** (-0.5)), 'Wv': _w((lambda *s: torch.randn(*s, dtype=torch.bfloat16) * 0.8)(Dm, H, Dh) * Dm ** (-0.5)), 'Wo': _w((lambda *s: torch.randn(*s, dtype=torch.bfloat16) * 0.8)(H, Dh, Dm) * (H * Dh) ** (-0.5)), 'seg_len': seg}
    _A_in = _A_setup()

    def _B_setup():
        B, S, D, F = (1, 2048, 1024, 3584)
        return {'x': _w((lambda *s: torch.randn(*s, dtype=torch.bfloat16) * 0.05)(B, S, D)), 'w1': _w((lambda *s: torch.randn(*s, dtype=torch.bfloat16) * 3.2)(D, F) * D ** (-0.5)), 'b1': _w((lambda *s: torch.randn(*s, dtype=torch.bfloat16) * 0.8)(F)), 'w2': _w((lambda *s: torch.randn(*s, dtype=torch.bfloat16) * 0.1)(F, D) * F ** (-0.5)), 'b2': _w((lambda *s: torch.randn(*s, dtype=torch.bfloat16) * 0.1)(D))}
    _B_in = _B_setup()
    return {'A_x': _A_in['x'], 'A_mem0': _A_in['mem0'], 'A_Wq': _A_in['Wq'], 'A_Wk': _A_in['Wk'], 'A_Wv': _A_in['Wv'], 'A_Wo': _A_in['Wo'], 'A_seg_len': _A_in['seg_len'], 'B_x': _B_in['x'], 'B_w1': _B_in['w1'], 'B_b1': _B_in['b1'], 'B_w2': _B_in['w2'], 'B_b2': _B_in['b2']}

def reference(A_x, A_mem0, A_Wq, A_Wk, A_Wv, A_Wo, A_seg_len, B_x, B_w1, B_b1, B_w2, B_b2):

    def _A_ref(x, mem0, Wq, Wk, Wv, Wo, seg_len):
        B, S, Dm = x.shape
        H, Dh = (Wq.shape[1], Wq.shape[2])
        M = mem0.shape[1]
        scale = Dh ** (-0.5)
        n_seg = S // seg_len
        L = M + seg_len
        idx = torch.arange(L)
        mask = (idx[:, None] < M) | (idx[:, None] >= idx[None, :])
        mem = mem0
        outs = []
        for s in range(n_seg):
            xs = x[:, s * seg_len:(s + 1) * seg_len, :].clone()
            h = torch.concatenate([mem, xs], axis=1)
            q = torch.einsum('btd,dhe->bhte', h, Wq)
            k = torch.einsum('btd,dhe->bhte', h, Wk)
            v = torch.einsum('btd,dhe->bhte', h, Wv)
            scores = torch.einsum('bhqe,bhke->bhqk', q, k) * scale
            scores = torch.where(mask[None, None], scores, -1000000000.0)
            p = _softmax(scores, axis=-1)
            o = torch.einsum('bhqk,bhke->bhqe', p, v)
            o = torch.einsum('bhte,hed->btd', o, Wo)
            mem = o[:, :M, :].clone()
            outs.append(o[:, M:, :])
        return torch.concatenate(outs, axis=1)

    def _B_ref(x, w1, b1, w2, b2):
        y = _layernorm(x, axis=-1)
        h = _gelu(torch.einsum('bsd,df->bsf', y, w1) + b1)
        return torch.einsum('bsf,fd->bsd', h, w2) + b2
    x = A_x
    x = x + _A_ref(_layernorm(x, axis=-1), A_mem0, A_Wq, A_Wk, A_Wv, A_Wo, A_seg_len)
    x = x + _B_ref(_layernorm(x, axis=-1), B_w1, B_b1, B_w2, B_b2)
    x = x + _A_ref(_layernorm(x, axis=-1), A_mem0, A_Wq, A_Wk, A_Wv, A_Wo, A_seg_len)
    x = x + _B_ref(_layernorm(x, axis=-1), B_w1, B_b1, B_w2, B_b2)
    return x
```

```cpp
#include <hip/hip_runtime.h>
#include <stdint.h>
#define LAS __attribute__((address_space(3)))
#define GAS __attribute__((address_space(1)))
typedef unsigned short bf16_t;
typedef short bf16x8 __attribute__((ext_vector_type(8)));
typedef short bf16x4 __attribute__((ext_vector_type(4)));
typedef float f32x4 __attribute__((ext_vector_type(4)));
typedef float f32x2 __attribute__((ext_vector_type(2)));
typedef unsigned u32x4 __attribute__((ext_vector_type(4)));
typedef unsigned u32x2 __attribute__((ext_vector_type(2)));
typedef __bf16 bf16v2 __attribute__((ext_vector_type(2)));

__device__ __forceinline__ unsigned cvt_pk_bf16(float lo, float hi) { f32x2 v = {lo, hi}; bf16v2 r = __builtin_convertvector(v, bf16v2); return __builtin_bit_cast(unsigned, r); }
__device__ __forceinline__ float rbf(float x) { return __uint_as_float(cvt_pk_bf16(x, x) << 16); }
__device__ __forceinline__ float bflo(unsigned w) { return __uint_as_float(w << 16); }
__device__ __forceinline__ float bfhi(unsigned w) { return __uint_as_float(w & 0xFFFF0000u); }

constexpr int S_ = 2048, DM = 1024, NH = 32, DH = 128, NSEG = 64, FF = 3584;
constexpr unsigned SENT = 0xFFFFFFFFu;

struct ChainArgs {
    const bf16_t* mem0; const bf16_t* Wq; const bf16_t* Wk; const bf16_t* Wv; const bf16_t* Wo;
    const bf16_t* qkv;
    unsigned* membuf;
    unsigned* Ubuf;
    unsigned* Xbuf;
    bf16_t* kvmem;
    unsigned* tmo;
    int lag; int nstep;
    unsigned qstride; unsigned pad0;
};
#define CH_WL 0
#define CH_OV (128*1024)
#define CH_LDS (160*1024)

__device__ __forceinline__ u32x4 ld16_sc1(__amdgpu_buffer_rsrc_t r, unsigned off) { asm volatile("" ::: "memory"); u32x4 v = __builtin_amdgcn_raw_buffer_load_b128(r, off, 0, 16); asm volatile("" ::: "memory"); return v; }
__device__ __forceinline__ void st16_sc1(u32x4 v, __amdgpu_buffer_rsrc_t r, unsigned off) { __builtin_amdgcn_raw_buffer_store_b128(v, r, off, 0, 16); }
__device__ __forceinline__ void st8b_sc1(u32x2 v, __amdgpu_buffer_rsrc_t r, unsigned off) { __builtin_amdgcn_raw_buffer_store_b64(v, r, off, 0, 16); }
__device__ __forceinline__ void st8_sc1(void* p, u32x2 v) { __hip_atomic_store((GAS unsigned long long*)p, ((unsigned long long)v.y << 32) | v.x, __ATOMIC_RELAXED, __HIP_MEMORY_SCOPE_AGENT); }
__device__ __forceinline__ void st4_sc1(unsigned* p, unsigned v) { __hip_atomic_store((GAS unsigned*)p, v, __ATOMIC_RELAXED, __HIP_MEMORY_SCOPE_AGENT); }

typedef short v4s __attribute__((ext_vector_type(4)));
__device__ __forceinline__ bf16x8 tr_frag_at(LAS unsigned char* a0) {
    v4s lo = __builtin_amdgcn_ds_read_tr16_b64_v4i16((LAS v4s*)a0);
    v4s hi = __builtin_amdgcn_ds_read_tr16_b64_v4i16((LAS v4s*)(a0 + 4 * 256));
    bf16x8 r; r[0] = lo[0]; r[1] = lo[1]; r[2] = lo[2]; r[3] = lo[3]; r[4] = hi[0]; r[5] = hi[1]; r[6] = hi[2]; r[7] = hi[3];
    return r;
}
template <int CTRL> __device__ __forceinline__ float dpp_f(float x) { return __int_as_float(__builtin_amdgcn_update_dpp(0, __float_as_int(x), CTRL, 0xF, 0xF, false)); }
__device__ __forceinline__ float row16_max(float x) { x = fmaxf(x, dpp_f<0x128>(x)); x = fmaxf(x, dpp_f<0x124>(x)); x = fmaxf(x, dpp_f<0x122>(x)); x = fmaxf(x, dpp_f<0x121>(x)); return x; }
__device__ __forceinline__ float row16_sum(float x) { x += dpp_f<0x128>(x); x += dpp_f<0x124>(x); x += dpp_f<0x122>(x); x += dpp_f<0x121>(x); return x; }

__device__ __forceinline__ void chain_role(const ChainArgs& a, LAS unsigned char* lds, int c) {
    const int tid = threadIdx.x, w = __builtin_amdgcn_readfirstlane(tid >> 6);
    const int h = c >> 1, hf = c & 1, kh = w >> 2;
    bf16x8 wqk[32], wv[16];
    {
        const int lane = tid & 63;
        if (w == 0) {
            const int tok = lane & 15, g4 = lane >> 4;
            u32x2 o = *(const u32x2*)(a.mem0 + (tok & 7) * 1024 + c * 16 + g4 * 4);
            if (o.x == SENT) o.x = 0x7FC07FC0u; if (o.y == SENT) o.y = 0x7FC07FC0u;
            st8_sc1((void*)(a.membuf + (c >> 1) * 256 + (2 * (c & 1) + (g4 >> 1)) * 64 + tok * 4 + (g4 & 1) * 2), o);
        }
        const int g = lane >> 4, q = (lane & 15) >> 2, p = lane & 3;
        const int row = tid >> 4, ch = tid & 15;
#pragma unroll
        for (int grp = 0; grp < 2; ++grp) {
            const bf16_t* src = (ch < 8 ? a.Wq : a.Wk) + (size_t)(grp * 512 + row) * 4096 + h * 128 + hf * 64 + (ch & 7) * 8;
#pragma unroll
            for (int i = 0; i < 16; ++i)
                __builtin_amdgcn_global_load_lds((const unsigned*)(src + (size_t)i * 32 * 4096), (LAS unsigned*)(lds + i * 8192 + w * 1024), 16, 0, 0);
            asm volatile("s_waitcnt vmcnt(0)" ::: "memory");
            __syncthreads();
            LAS unsigned char* a0 = lds + (8 * g + q) * 256 + ((p < 2 ? 8 * w + 4 * p : 64 + 8 * w + 4 * (p - 2))) * 2;
            if (grp == kh) {
#pragma unroll
                for (int i = 0; i < 16; ++i) wqk[i] = tr_frag_at(a0 + i * 8192);
            } else {
#pragma unroll
                for (int i = 0; i < 16; ++i) wqk[16 + i] = tr_frag_at(a0 + i * 8192);
            }
            asm volatile("s_waitcnt lgkmcnt(0)" ::: "memory");
            __syncthreads();
        }
        {
            const bf16_t* src = a.Wv + (size_t)((ch >> 3) * 512 + row) * 4096 + h * 128 + hf * 64 + (ch & 7) * 8;
#pragma unroll
            for (int i = 0; i < 16; ++i)
                __builtin_amdgcn_global_load_lds((const unsigned*)(src + (size_t)i * 32 * 4096), (LAS unsigned*)(lds + i * 8192 + w * 1024), 16, 0, 0);
            asm volatile("s_waitcnt vmcnt(0)" ::: "memory");
            __syncthreads();
            LAS unsigned char* a0 = lds + (8 * g + q) * 256 + (64 * kh + 16 * (w & 3) + 4 * p) * 2;
#pragma unroll
            for (int i = 0; i < 16; ++i) wv[i] = tr_frag_at(a0 + i * 8192);
            asm volatile("s_waitcnt lgkmcnt(0)" ::: "memory");
            __syncthreads();
        }
#pragma unroll 1
        for (int cg = 0; cg < 4; ++cg) {
            const bf16_t* src = a.Wo + (size_t)(h * 128 + row) * 1024 + hf * 512 + cg * 128 + ch * 8;
#pragma unroll
            for (int ks = 0; ks < 4; ++ks)
                __builtin_amdgcn_global_load_lds((const unsigned*)(src + (size_t)ks * 32 * 1024), (LAS unsigned*)(lds + CH_OV + ks * 8192 + w * 1024), 16, 0, 0);
            asm volatile("s_waitcnt vmcnt(0)" ::: "memory");
            __syncthreads();
            LAS unsigned char* a0 = lds + CH_OV + (8 * g + q) * 256 + (16 * w + 4 * p) * 2;
#pragma unroll
            for (int ks = 0; ks < 4; ++ks) { bf16x8 f = tr_frag_at(a0 + ks * 8192);
                *(LAS bf16x8*)(lds + CH_WL + w * 16384 + (4 * cg + ks) * 1024 + lane * 16) = f; }
            asm volatile("s_waitcnt lgkmcnt(0)" ::: "memory");
            __syncthreads();
        }
    }
    const int T = a.nstep, L = a.lag;
    __amdgpu_buffer_rsrc_t rs_mem = __builtin_amdgcn_make_buffer_rsrc((void*)a.membuf, 0, T * 32768, 0x00020000);
    __amdgpu_buffer_rsrc_t rs_U = __builtin_amdgcn_make_buffer_rsrc((void*)a.Ubuf, 0, 64 * 32768, 0x00020000);
    __amdgpu_buffer_rsrc_t rs_X = __builtin_amdgcn_make_buffer_rsrc((void*)a.Xbuf, 0, 64 * 8192, 0x00020000);
    __amdgpu_buffer_rsrc_t rs_q = __builtin_amdgcn_make_buffer_rsrc((void*)a.qkv, 0, a.qstride + 2048u * 24576u, 0x00020000);
    __amdgpu_buffer_rsrc_t rs_kv = __builtin_amdgcn_make_buffer_rsrc((void*)a.kvmem, 0, 4 * 64 * 32 * 2048, 0x00020000);
    LAS unsigned char* ov = lds + CH_OV;
    bool dead = false;
#pragma unroll 1
    for (int t = 0; t < T; ++t) {
        int lane = tid & 63; asm volatile("" : "+v"(lane));
        const int tok = lane & 15, g4 = lane >> 4, slot = tok >> 3;
        const int s0 = t < 64 ? t : 63, s1 = t - L < 0 ? 0 : (t - L > 63 ? 63 : t - L);
        const bool act0 = t < 64, act1 = t >= L && t - L < 64;
        const int sseg = slot ? s1 : s0; const bool act = slot ? act1 : act0;
        const unsigned tag = t & 1;
        const bool last = (t == T - 1);
        u32x2 kx[2];
        const unsigned qo0 = act0 ? (unsigned)s0 * 786432u + h * 768 : 0x80000000u, qo1 = act1 ? (unsigned)s1 * 786432u + a.qstride + h * 768 : 0x80000000u;
        {   const unsigned kp = ((g4 >> 1) ? qo1 : qo0) + tok * 24576 + (128 + hf * 64 + 8 * w + 4 * (g4 & 1)) * 2;
            kx[0] = __builtin_amdgcn_raw_buffer_load_b64(rs_q, kp, 0, 0); kx[1] = __builtin_amdgcn_raw_buffer_load_b64(rs_q, kp + 16 * 24576, 0, 0); }
        {
            u32x4 v[4];
            {
                unsigned spins = 0;
                for (;;) {
                    bool ok = true;
#pragma unroll
                    for (int i = 0; i < 4; ++i) v[i] = ld16_sc1(rs_mem, t * 32768 + w * 4096 + i * 1024 + lane * 16);
#pragma unroll
                    for (int i = 0; i < 4; ++i) ok = ok && v[i].x != SENT && v[i].y != SENT && v[i].z != SENT && v[i].w != SENT;
                    if (__all(ok)) break;
                    if (dead || ++spins > 200000u) { dead = true; if (lane == 0) st4_sc1(a.tmo, 1u); break; }
                }
            }
#pragma unroll
            for (int i = 0; i < 4; ++i) *(LAS u32x4*)(ov + w * 4096 + i * 1024 + lane * 16) = v[i];
        }
        __syncthreads();
        f32x4 aqk = {0.f, 0.f, 0.f, 0.f}, av = {0.f, 0.f, 0.f, 0.f};
        {
            LAS unsigned char* mf = ov + lane * 16;
            const int kofs = kh * 16384;
            bf16x8 bb[3];
            bb[0] = *(LAS bf16x8*)(mf + kofs); bb[1] = *(LAS bf16x8*)(mf + kofs + 1024);
#pragma unroll
            for (int i = 0; i < 32; ++i) {
                if (i + 2 < 32) bb[(i + 2) % 3] = *(LAS bf16x8*)(mf + (((i + 2) * 1024) & 16383) + (i + 2 < 16 ? kofs : (16384 - kofs)));
                __builtin_amdgcn_sched_barrier(0);
                aqk = __builtin_amdgcn_mfma_f32_16x16x32_bf16(wqk[i], bb[i % 3], aqk, 0, 0, 0);
                if (i < 16) av = __builtin_amdgcn_mfma_f32_16x16x32_bf16(wv[i], bb[i % 3], av, 0, 0, 0);
                __builtin_amdgcn_sched_barrier(0);
            }
        }
        u32x2 x2; x2.x = cvt_pk_bf16(aqk[0], aqk[1]); x2.y = cvt_pk_bf16(aqk[2], aqk[3]);
        const unsigned kvo = (unsigned)(((slot * 2) * 64 + sseg) * 32 + h) * 2048 + (tok & 7) * 256 + hf * 128;
        if (act && g4 >= 2) st8b_sc1(x2, rs_kv, kvo + 16 * w + 8 * (g4 - 2));
        if (last) {
        }
        f32x4 sm, sx0, sx1;
        {
            auto r0 = __builtin_amdgcn_permlane32_swap(x2.x, x2.x, false, false);
            auto r1 = __builtin_amdgcn_permlane32_swap(x2.y, x2.y, false, false);
            u32x2 qa, kb; qa.x = r0[0]; qa.y = r1[0]; kb.x = r0[1]; kb.y = r1[1];
            if ((g4 >> 1) != slot) { qa.x = 0u; qa.y = 0u; }
            const f32x4 z = {0.f, 0.f, 0.f, 0.f};
            sm = __builtin_amdgcn_mfma_f32_16x16x16bf16_1k(__builtin_bit_cast(bf16x4, qa), __builtin_bit_cast(bf16x4, kb), z, 0, 0, 0);
            sx0 = __builtin_amdgcn_mfma_f32_16x16x16bf16_1k(__builtin_bit_cast(bf16x4, qa), __builtin_bit_cast(bf16x4, kx[0]), z, 0, 0, 0);
            sx1 = __builtin_amdgcn_mfma_f32_16x16x16bf16_1k(__builtin_bit_cast(bf16x4, qa), __builtin_bit_cast(bf16x4, kx[1]), z, 0, 0, 0);
        }
        u32x4 vx0, vx1;
        if (!last) { const unsigned vp = (kh ? qo1 : qo0) + ((w & 3) * 8 + (lane >> 3)) * 24576 + 512 + (lane & 7) * 32;
            vx0 = __builtin_amdgcn_raw_buffer_load_b128(rs_q, vp, 0, 0); vx1 = __builtin_amdgcn_raw_buffer_load_b128(rs_q, vp + 16, 0, 0); }
        __syncthreads();
        if (!last) {
            LAS float* sp = (LAS float*)(ov + w * 2560);
            if ((g4 >> 1) == slot) {
#pragma unroll
                for (int jj = 0; jj < 4; ++jj) sp[(4 * g4 + jj) * 40 + (tok & 7)] = sm[jj];
            }
#pragma unroll
            for (int jj = 0; jj < 4; ++jj) { sp[(4 * g4 + jj) * 40 + 8 + tok] = sx0[jj]; sp[(4 * g4 + jj) * 40 + 24 + tok] = sx1[jj]; }
        }
        if (w < 4) *(LAS f32x4*)(ov + 20480 + w * 1024 + lane * 16) = av;
        __syncthreads();
        f32x4 own;
        u32x2 vb;
        if (w >= 4) {
            av += *(LAS f32x4*)(ov + 20480 + (w - 4) * 1024 + lane * 16);
            vb.x = cvt_pk_bf16(av[0], av[1]); vb.y = cvt_pk_bf16(av[2], av[3]);
            if (!last) { u32x4 o; o.x = (vb.x << 16) | tag; o.y = (vb.x & 0xFFFF0000u) | tag; o.z = (vb.y << 16) | tag; o.w = (vb.y & 0xFFFF0000u) | tag;
                st16_sc1(o, rs_X, (unsigned)(c * 8192 + 4096 + (w - 4) * 1024 + lane * 16)); }
            if (act) st8b_sc1(vb, rs_kv, kvo + 64 * 32 * 2048 + 32 * (w - 4) + 8 * g4);
        } else if (!last) {
            const int qt = 4 * w + g4, gr = tok < 10 ? tok : 9;
            own = (f32x4){0.f, 0.f, 0.f, 0.f};
#pragma unroll
            for (int ww = 0; ww < 8; ++ww) own += *(LAS f32x4*)(ov + ww * 2560 + qt * 160 + gr * 16);
            u32x4 o; o.x = (__float_as_uint(own[0]) & ~1u) | tag; o.y = (__float_as_uint(own[1]) & ~1u) | tag; o.z = (__float_as_uint(own[2]) & ~1u) | tag; o.w = (__float_as_uint(own[3]) & ~1u) | tag;
            if (tok < 10) st16_sc1(o, rs_X, (unsigned)(c * 8192 + qt * 160 + tok * 16));
            own[0] = __uint_as_float(o.x & ~1u); own[1] = __uint_as_float(o.y & ~1u); own[2] = __uint_as_float(o.z & ~1u); own[3] = __uint_as_float(o.w & ~1u);
        }
        if (last) break;
        __syncthreads();
        {   LAS unsigned char* d = ov + (w >> 2) * 10240 + (8 + (w & 3) * 8 + (lane >> 3)) * 256 + (lane & 7) * 32;
            *(LAS u32x4*)d = vx0; *(LAS u32x4*)(d + 16) = vx1; }
        if (w >= 4) {
            *(LAS u32x2*)(ov + slot * 10240 + (tok & 7) * 256 + (hf * 64 + 16 * (w - 4) + 4 * g4) * 2) = vb;
            u32x4 pv; unsigned spins = 0;
            for (;;) {
                pv = ld16_sc1(rs_X, (unsigned)((c ^ 1) * 8192 + 4096 + (w - 4) * 1024 + lane * 16));
                if (__all(((pv.x ^ tag) | (pv.y ^ tag) | (pv.z ^ tag) | (pv.w ^ tag)) & 1u ? false : true)) break;
                if (dead || ++spins > 200000u) { dead = true; if (lane == 0) st4_sc1(a.tmo, 3u); break; }
            }
            u32x2 o; o.x = (pv.x >> 16) | (pv.y & 0xFFFF0000u); o.y = (pv.z >> 16) | (pv.w & 0xFFFF0000u);
            *(LAS u32x2*)(ov + slot * 10240 + (tok & 7) * 256 + ((hf ^ 1) * 64 + 16 * (w - 4) + 4 * g4) * 2) = o;
        } else {
            const int qt = 4 * w + g4, gr = tok < 10 ? tok : 9;
            u32x4 ps; unsigned spins = 0;
            for (;;) {
                ps = ld16_sc1(rs_X, (unsigned)((c ^ 1) * 8192 + qt * 160 + gr * 16));
                if (__all(((ps.x ^ tag) | (ps.y ^ tag) | (ps.z ^ tag) | (ps.w ^ tag)) & 1u ? false : true)) break;
                if (dead || ++spins > 200000u) { dead = true; if (lane == 0) st4_sc1(a.tmo, 4u); break; }
            }
            float sv[4]; float m = -3.0e38f;
#pragma unroll
            for (int j = 0; j < 4; ++j) { const float s = own[j] + __uint_as_float(ps[j] & ~1u); sv[j] = rbf(rbf(s) * 0.08838834764831845f); m = fmaxf(m, sv[j]); }
            if (tok >= 10) m = -3.0e38f;
            m = row16_max(m);
            float e[4], d = 0.f;
#pragma unroll
            for (int j = 0; j < 4; ++j) { e[j] = tok < 10 ? __expf(sv[j] - m) : 0.f; d += e[j]; }
            d = row16_sum(d);
            const float inv = 1.0f / d;
            u32x2 pw; pw.x = cvt_pk_bf16(e[0] * inv, e[1] * inv); pw.y = cvt_pk_bf16(e[2] * inv, e[3] * inv);
            if (tok < 12) *(LAS u32x2*)(ov + 20480 + qt * 96 + tok * 8) = pw;
        }
        __syncthreads();
        {
            const u32x4 p0 = *(LAS u32x4*)(ov + 20480 + tok * 96 + g4 * 16);
            const u32x2 p1 = *(LAS u32x2*)(ov + 20480 + tok * 96 + 64 + g4 * 8);
            f32x4 oa = {0.f, 0.f, 0.f, 0.f};
            const int q4 = (lane & 15) >> 2, p4 = lane & 3;
#pragma unroll
            for (int cc = 0; cc < 2; ++cc) {
                LAS unsigned char* a0 = ov + cc * 10240 + (8 * g4 + q4) * 256 + (16 * w + 4 * p4) * 2;
                v4s lo = __builtin_amdgcn_ds_read_tr16_b64_v4i16((LAS v4s*)a0);
                v4s hi = __builtin_amdgcn_ds_read_tr16_b64_v4i16((LAS v4s*)(a0 + 4 * 256));
                int r2 = 32 + 4 * g4 + q4; if (r2 > 39) r2 = 39;
                v4s l2 = __builtin_amdgcn_ds_read_tr16_b64_v4i16((LAS v4s*)(ov + cc * 10240 + r2 * 256 + (16 * w + 4 * p4) * 2));
                bf16x8 vf; vf[0] = lo[0]; vf[1] = lo[1]; vf[2] = lo[2]; vf[3] = lo[3]; vf[4] = hi[0]; vf[5] = hi[1]; vf[6] = hi[2]; vf[7] = hi[3];
                u32x4 pa = p0; u32x2 pb = p1; if (slot != cc) { pa = (u32x4){0u, 0u, 0u, 0u}; pb = (u32x2){0u, 0u}; }
                oa = __builtin_amdgcn_mfma_f32_16x16x32_bf16(vf, __builtin_bit_cast(bf16x8, pa), oa, 0, 0, 0);
                oa = __builtin_amdgcn_mfma_f32_16x16x16bf16_1k(l2, __builtin_bit_cast(bf16x4, pb), oa, 0, 0, 0);
            }
            u32x2 ob; ob.x = cvt_pk_bf16(oa[0], oa[1]); ob.y = cvt_pk_bf16(oa[2], oa[3]);
            *(LAS u32x2*)(ov + 24576 + (w >> 1) * 1024 + (2 * (w & 1) + (g4 >> 1)) * 256 + tok * 16 + (g4 & 1) * 8) = ob;
        }
        __syncthreads();
        {
            bf16x8 of[4];
#pragma unroll
            for (int ks = 0; ks < 4; ++ks) of[ks] = *(LAS bf16x8*)(ov + 24576 + ks * 1024 + lane * 16);
            LAS unsigned char* wl = lds + CH_WL + w * 16384 + lane * 16;
#pragma unroll
            for (int cg = 0; cg < 4; ++cg) {
                f32x4 u = {0.f, 0.f, 0.f, 0.f};
#pragma unroll
                for (int ks = 0; ks < 4; ++ks) u = __builtin_amdgcn_mfma_f32_16x16x32_bf16(*(LAS bf16x8*)(wl + (cg * 4 + ks) * 1024), of[ks], u, 0, 0, 0);
                u32x4 o; o.x = (__float_as_uint(u[0]) & ~1u) | tag; o.y = (__float_as_uint(u[1]) & ~1u) | tag; o.z = (__float_as_uint(u[2]) & ~1u) | tag; o.w = (__float_as_uint(u[3]) & ~1u) | tag;
                const int dt = hf * 32 + 8 * cg + w;
                st16_sc1(o, rs_U, (unsigned)((dt * 32 + h) * 1024 + tok * 64 + g4 * 16));
            }
        }
        {
            u32x4 uu[4]; unsigned spins = 0;
            const unsigned uoff = (unsigned)(c * 32768 + (4 * w) * 1024 + tok * 64 + g4 * 16);
            for (;;) {
#pragma unroll
                for (int i = 0; i < 4; ++i) uu[i] = ld16_sc1(rs_U, uoff + i * 1024);
                const unsigned m = tag ? 0xFFFFFFFFu : 0u; unsigned x = 0;
#pragma unroll
                for (int i = 0; i < 4; ++i) x |= (uu[i].x ^ m) | (uu[i].y ^ m) | (uu[i].z ^ m) | (uu[i].w ^ m);
                if (__all((x & 1u) == 0u)) break;
                if (dead || ++spins > 200000u) { dead = true; if (lane == 0) st4_sc1(a.tmo, 2u); break; }
            }
            f32x4 r0;
#pragma unroll
            for (int j = 0; j < 4; ++j) r0[j] = (__uint_as_float(uu[0][j] & ~1u) + __uint_as_float(uu[1][j] & ~1u)) + (__uint_as_float(uu[2][j] & ~1u) + __uint_as_float(uu[3][j] & ~1u));
            LAS unsigned char* red = ov;
            *(LAS f32x4*)(red + w * 1024 + lane * 16) = r0;
            __syncthreads();
            if (w == 0) {
                f32x4 tt = {0.f, 0.f, 0.f, 0.f};
#pragma unroll
                for (int ww = 0; ww < 8; ++ww) tt += *(LAS f32x4*)(red + ww * 1024 + lane * 16);
                u32x2 o; o.x = cvt_pk_bf16(tt[0], tt[1]); o.y = cvt_pk_bf16(tt[2], tt[3]);
                if (t + 1 == L && tok >= 8) o = *(const u32x2*)(a.mem0 + (tok & 7) * 1024 + c * 16 + g4 * 4);
                if (o.x == SENT) o.x = 0x7FC07FC0u; if (o.y == SENT) o.y = 0x7FC07FC0u;
                st8b_sc1(o, rs_mem, (unsigned)((t + 1) * 32768 + (c >> 1) * 1024 + (2 * (c & 1) + (g4 >> 1)) * 256 + tok * 16 + (g4 & 1) * 8));
            }
        }
    }
}

__global__ __launch_bounds__(512, 2) void chain_kernel(ChainArgs a) {
    extern __shared__ __attribute__((aligned(16))) unsigned char shm[];
    LAS unsigned char* lds = (LAS unsigned char*)shm;
    chain_role(a, lds, blockIdx.x);
}

__host__ __device__ __forceinline__ int lds_byte(int r, int c) { const int st = (r >> 4) * 2 + (c >> 5), rr = r & 15, cc = c & 31, ob = rr * 64 + cc * 2; return st * 1024 + (ob ^ (((ob >> 9) & 1) << 5)); }
__host__ __device__ __forceinline__ void stage_rc(int b, int& R, int& C) { const int st = b / 1024, sb = b % 1024, swz = sb ^ (((sb >> 9) & 1) << 5); R = (st >> 1) * 16 + swz / 64; C = (st & 1) * 32 + (swz % 64) / 2; }
__host__ __device__ __forceinline__ int perm32(int rho) { const int n = rho >> 4, i = rho & 15; return 8 * (i >> 2) + 4 * n + (i & 3); }

template <int N_> __device__ __forceinline__ void wait_v() {
    if constexpr (N_ == 0) asm volatile("s_waitcnt vmcnt(0)" ::: "memory");
    else if constexpr (N_ == 1) asm volatile("s_waitcnt vmcnt(1)" ::: "memory");
    else if constexpr (N_ == 2) asm volatile("s_waitcnt vmcnt(2)" ::: "memory");
    else if constexpr (N_ == 3) asm volatile("s_waitcnt vmcnt(3)" ::: "memory");
    else if constexpr (N_ == 4) asm volatile("s_waitcnt vmcnt(4)" ::: "memory");
    else if constexpr (N_ == 5) asm volatile("s_waitcnt vmcnt(5)" ::: "memory");
    else if constexpr (N_ == 6) asm volatile("s_waitcnt vmcnt(6)" ::: "memory");
}

template <int MT, bool PERM>
__device__ __forceinline__ void gemm_tile(LAS unsigned char* lds, const bf16_t* A, int lda, const bf16_t* Bt, int ldb, int nt, f32x4 (&acc)[2][2][MT][2]) {
    constexpr int NA = MT / 2;
    constexpr int HA = MT * 32;
    constexpr int HTA = HA * 128;
    constexpr int HTB = 16384;
    const int tid = threadIdx.x, wid = __builtin_amdgcn_readfirstlane(tid >> 6), lane = tid & 63, wr = wid >> 2, wc = wid & 3, fr = lane & 15, fq = lane >> 4;
    unsigned voffA[NA], voffB[2];
#pragma unroll
    for (int i = 0; i < NA; ++i) { int R, C; stage_rc(tid * 16 + i * 8192, R, C); voffA[i] = (unsigned)(R * lda + C) * 2u; }
#pragma unroll
    for (int i = 0; i < 2; ++i) { int R, C; stage_rc(tid * 16 + i * 8192, R, C); const int Rb = PERM ? ((R & ~31) + perm32(R & 31)) : R; voffB[i] = (unsigned)(Rb * ldb + C) * 2u; }
    const size_t kstep = 128;
    const size_t hstepA = (size_t)HA * lda * 2, hstepB = (size_t)128 * ldb * 2;
    const unsigned ldsw = (unsigned)wid * 1024u;
    const int aoff = lds_byte(wr * MT * 16 + fr, fq * 8), boff = lds_byte(wc * 32 + fr, fq * 8);
    const char* cA = (const char*)A; const char* cB = (const char*)Bt;
#define SA_(b, h) (((b) * 2 + (h)) * HTA)
#define SB_(b, h) (4 * HTA + ((b) * 2 + (h)) * HTB)
#define STAGE_A(b, h, kt) do { _Pragma("unroll") for (int _i = 0; _i < NA; ++_i) \
        __builtin_amdgcn_global_load_lds((const unsigned*)(cA + (h) * hstepA + (size_t)(kt) * kstep + voffA[_i]), (LAS unsigned*)(lds + SA_(b, h) + ldsw + _i * 8192), 16, 0, 0); } while (0)
#define STAGE_B(b, h, kt) do { _Pragma("unroll") for (int _i = 0; _i < 2; ++_i) \
        __builtin_amdgcn_global_load_lds((const unsigned*)(cB + (h) * hstepB + (size_t)(kt) * kstep + voffB[_i]), (LAS unsigned*)(lds + SB_(b, h) + ldsw + _i * 8192), 16, 0, 0); } while (0)
#define LDA_(dst, b, h) do { _Pragma("unroll") for (int m = 0; m < MT; ++m) _Pragma("unroll") for (int k = 0; k < 2; ++k) dst[m][k] = *(const LAS bf16x8*)(lds + SA_(b, h) + aoff + m * 2048 + k * 1024); } while (0)
#define LDB_(dst, b, h) do { _Pragma("unroll") for (int n = 0; n < 2; ++n) _Pragma("unroll") for (int k = 0; k < 2; ++k) dst[n][k] = *(const LAS bf16x8*)(lds + SB_(b, h) + boff + n * 2048 + k * 1024); } while (0)
#define MMA_(ai, bj, At, Bq) do { __builtin_amdgcn_s_setprio(1); _Pragma("unroll") for (int m = 0; m < MT; ++m) _Pragma("unroll") for (int n = 0; n < 2; ++n) _Pragma("unroll") for (int k = 0; k < 2; ++k) \
        acc[ai][bj][m][n] = __builtin_amdgcn_mfma_f32_16x16x32_bf16(Bq[n][k], At[m][k], acc[ai][bj][m][n], 0, 0, 0); __builtin_amdgcn_s_setprio(0); } while (0)
#define WAIT_L(n) asm volatile("s_waitcnt lgkmcnt(" #n ")" ::: "memory")
#define BAR __builtin_amdgcn_s_barrier()
#define SCHED __builtin_amdgcn_sched_barrier(0)
#pragma unroll
    for (int a = 0; a < 2; ++a)
#pragma unroll
        for (int b = 0; b < 2; ++b)
#pragma unroll
            for (int m = 0; m < MT; ++m)
#pragma unroll
                for (int n = 0; n < 2; ++n) acc[a][b][m][n] = (f32x4){0.f, 0.f, 0.f, 0.f};
    bf16x8 At[MT][2], B0[2][2], B1[2][2];
    STAGE_B(0, 0, 0); STAGE_A(0, 0, 0); STAGE_B(0, 1, 0); STAGE_A(0, 1, 0);
    if (wr == 1) BAR;
    wait_v<2 + NA>(); BAR;
    STAGE_B(1, 0, 1); STAGE_A(1, 0, 1); STAGE_B(1, 1, 1);
    wait_v<4 + NA>(); BAR;
    for (int t = 0; t < nt - 2; t += 2) {
        LDB_(B0, 0, 0); SCHED; LDA_(At, 0, 0); STAGE_A(1, 1, t + 1);
        if constexpr (MT == 4) WAIT_L(8); else WAIT_L(4);
        BAR; WAIT_L(0); MMA_(0, 0, At, B0); BAR; SCHED;
        LDB_(B1, 0, 1); STAGE_B(0, 0, t + 2);
        BAR; WAIT_L(0); MMA_(0, 1, At, B1); BAR;
        LDA_(At, 0, 1); STAGE_A(0, 0, t + 2);
        BAR; WAIT_L(0); MMA_(1, 0, At, B0); BAR; SCHED;
        STAGE_B(0, 1, t + 2);
        wait_v<4 + NA>(); BAR; MMA_(1, 1, At, B1); BAR;
        LDB_(B0, 1, 0); SCHED; LDA_(At, 1, 0); STAGE_A(0, 1, t + 2);
        if constexpr (MT == 4) WAIT_L(8); else WAIT_L(4);
        BAR; WAIT_L(0); MMA_(0, 0, At, B0); BAR; SCHED;
        LDB_(B1, 1, 1); STAGE_B(1, 0, t + 3);
        BAR; WAIT_L(0); MMA_(0, 1, At, B1); BAR;
        LDA_(At, 1, 1); STAGE_A(1, 0, t + 3);
        BAR; WAIT_L(0); MMA_(1, 0, At, B0); BAR; SCHED;
        STAGE_B(1, 1, t + 3);
        wait_v<4 + NA>(); BAR; MMA_(1, 1, At, B1); BAR;
    }
    { LDB_(B0, 0, 0); wait_v<0>(); LDA_(At, 0, 0); STAGE_A(1, 1, nt - 1);
      BAR; WAIT_L(0); MMA_(0, 0, At, B0); BAR;
      LDB_(B1, 0, 1); BAR; WAIT_L(0); MMA_(0, 1, At, B1); BAR;
      LDA_(At, 0, 1); BAR; WAIT_L(0); MMA_(1, 0, At, B0); MMA_(1, 1, At, B1); BAR; }
    { LDB_(B0, 1, 0); LDA_(At, 1, 0); BAR; WAIT_L(0); MMA_(0, 0, At, B0); BAR;
      LDB_(B1, 1, 1); wait_v<0>(); BAR; WAIT_L(0); MMA_(0, 1, At, B1); BAR;
      LDA_(At, 1, 1); BAR; WAIT_L(0); MMA_(1, 0, At, B0); MMA_(1, 1, At, B1); BAR; }
    if (wr == 0) BAR;
#undef SA_
#undef SB_
#undef STAGE_A
#undef STAGE_B
#undef LDA_
#undef LDB_
#undef MMA_
}

__device__ __forceinline__ float gelu_tanh(float x) {
    const float u = 0.7978845608028654f * (x + 0.044715f * x * x * x);
    const float e = __expf(-2.0f * u);
    return x * __builtin_amdgcn_rcpf(1.0f + e);
}

template <int MT, int MODE>
__global__ __launch_bounds__(512, 2) void gemm_kernel(const bf16_t* __restrict__ A, int lda, const bf16_t* __restrict__ Bt, int ldb, void* __restrict__ C, int ldc, const bf16_t* __restrict__ bias,
                                                      int nM, int nN, int nt, size_t slab_stride) {
    extern __shared__ __attribute__((aligned(16))) unsigned char shm[];
    LAS unsigned char* lds = (LAS unsigned char*)shm;
    constexpr int BM = 64 * MT;
    const int ntile = nM * nN;
    const int ks = blockIdx.x / ntile; int t = blockIdx.x % ntile;
    const int pm = t % nM, pn = t / nM;
    f32x4 acc[2][2][MT][2];
    gemm_tile<MT, MODE != 2>(lds, A + (size_t)pm * BM * lda + (size_t)ks * nt * 64, lda, Bt + (size_t)pn * 256 * ldb + (size_t)ks * nt * 64, ldb, nt, acc);
    const int tid = threadIdx.x, wid = tid >> 6, lane = tid & 63, wr = wid >> 2, wc = wid & 3, fr = lane & 15, fq = lane >> 4;
    const int row0 = pm * BM + wr * MT * 16 + fr;
    if constexpr (MODE == 2) {
        float* Cf = (float*)C + (size_t)ks * slab_stride; const int col0 = pn * 256 + wc * 32 + 4 * fq;
#pragma unroll
        for (int ai = 0; ai < 2; ++ai)
#pragma unroll
            for (int m = 0; m < MT; ++m) { float* rowp = Cf + (size_t)(row0 + ai * MT * 32 + m * 16) * ldc + col0;
#pragma unroll
                for (int bj = 0; bj < 2; ++bj)
#pragma unroll
                    for (int n = 0; n < 2; ++n) *(f32x4*)(rowp + bj * 128 + n * 16) = acc[ai][bj][m][n]; }
    } else {
        bf16_t* O = (bf16_t*)C; const int col0 = pn * 256 + wc * 32 + 8 * fq;
        float bv[2][8];
        if constexpr (MODE == 1) {
#pragma unroll
            for (int bj = 0; bj < 2; ++bj) { u32x4 b = *(const u32x4*)(bias + col0 + bj * 128);
                bv[bj][0] = bflo(b.x); bv[bj][1] = bfhi(b.x); bv[bj][2] = bflo(b.y); bv[bj][3] = bfhi(b.y); bv[bj][4] = bflo(b.z); bv[bj][5] = bfhi(b.z); bv[bj][6] = bflo(b.w); bv[bj][7] = bfhi(b.w); }
        }
#pragma unroll
        for (int ai = 0; ai < 2; ++ai)
#pragma unroll
            for (int m = 0; m < MT; ++m) { bf16_t* rowp = O + (size_t)(row0 + ai * MT * 32 + m * 16) * ldc + col0;
#pragma unroll
                for (int bj = 0; bj < 2; ++bj) { f32x4 v0 = acc[ai][bj][m][0], v1 = acc[ai][bj][m][1];
                    if constexpr (MODE == 1) {
#pragma unroll
                        for (int j = 0; j < 4; ++j) { v0[j] = gelu_tanh(rbf(rbf(v0[j]) + bv[bj][j])); v1[j] = gelu_tanh(rbf(rbf(v1[j]) + bv[bj][4 + j])); }
                    }
                    u32x4 w; w.x = cvt_pk_bf16(v0[0], v0[1]); w.y = cvt_pk_bf16(v0[2], v0[3]); w.z = cvt_pk_bf16(v1[0], v1[1]); w.w = cvt_pk_bf16(v1[2], v1[3]);
                    *(u32x4*)(rowp + bj * 128) = w; } }
    }
}

__global__ __launch_bounds__(256) void transpose_kernel(const bf16_t* __restrict__ W, bf16_t* __restrict__ Wt, int K, int N, int which) {
    __shared__ bf16_t tile[64][72];
    const int nb = N / 64, bk = blockIdx.x / nb, bn = blockIdx.x % nb, t = threadIdx.x;
#pragma unroll
    for (int i = 0; i < 2; ++i) { const int r = (t >> 3) + i * 32, cc = (t & 7) * 8;
        u32x4 v = *(const u32x4*)(W + (size_t)(bk * 64 + r) * N + bn * 64 + cc);
        *(u32x4*)&tile[r][cc] = v; }
    __syncthreads();
#pragma unroll
    for (int i = 0; i < 2; ++i) { const int n = (t >> 3) + i * 32, kc = (t & 7) * 8;
        unsigned short e[8];
#pragma unroll
        for (int j = 0; j < 8; ++j) e[j] = tile[kc + j][n];
        u32x4 v; v.x = e[0] | ((unsigned)e[1] << 16); v.y = e[2] | ((unsigned)e[3] << 16); v.z = e[4] | ((unsigned)e[5] << 16); v.w = e[6] | ((unsigned)e[7] << 16);
        int ng = bn * 64 + n; if (which >= 0) ng = (ng >> 7) * 384 + which * 128 + (ng & 127);
        *(u32x4*)(Wt + (size_t)ng * K + bk * 64 + kc) = v; }
}

__device__ __forceinline__ float wave_sum(float v) {
#pragma unroll
    for (int o = 32; o >= 1; o >>= 1) v += __shfl_xor(v, o);
    return v;
}
__device__ __forceinline__ void ln16(const float (&x)[16], bf16_t* dst, int lane) {
    float s = 0.f;
#pragma unroll
    for (int j = 0; j < 16; ++j) s += x[j];
    const float mean = wave_sum(s) * (1.0f / 1024.0f);
    float q = 0.f;
#pragma unroll
    for (int j = 0; j < 16; ++j) { const float d = x[j] - mean; q += d * d; }
    const float var = rbf(wave_sum(q) * (1.0f / 1024.0f));
    const float mb = rbf(mean);
    const float sd = rbf(sqrtf(rbf(var + 1e-5f)));
    const float inv = 1.0f / sd;
    float y[16];
#pragma unroll
    for (int j = 0; j < 16; ++j) y[j] = rbf(x[j] - mb) * inv;
    u32x4 a, b;
    a.x = cvt_pk_bf16(y[0], y[1]); a.y = cvt_pk_bf16(y[2], y[3]); a.z = cvt_pk_bf16(y[4], y[5]); a.w = cvt_pk_bf16(y[6], y[7]);
    b.x = cvt_pk_bf16(y[8], y[9]); b.y = cvt_pk_bf16(y[10], y[11]); b.z = cvt_pk_bf16(y[12], y[13]); b.w = cvt_pk_bf16(y[14], y[15]);
    *(u32x4*)(dst + lane * 8) = a; *(u32x4*)(dst + 512 + lane * 8) = b;
}
__device__ __forceinline__ void unpack8(u32x4 v, float* x) { x[0] = bflo(v.x); x[1] = bfhi(v.x); x[2] = bflo(v.y); x[3] = bfhi(v.y); x[4] = bflo(v.z); x[5] = bfhi(v.z); x[6] = bflo(v.w); x[7] = bfhi(v.w); }

__global__ __launch_bounds__(256) void ln_kernel(const bf16_t* __restrict__ x, bf16_t* __restrict__ xn) {
    const int row = blockIdx.x * 4 + (threadIdx.x >> 6), lane = threadIdx.x & 63;
    float v[16];
    unpack8(*(const u32x4*)(x + (size_t)row * DM + lane * 8), v); unpack8(*(const u32x4*)(x + (size_t)row * DM + 512 + lane * 8), v + 8);
    ln16(v, xn + (size_t)row * DM, lane);
}
__global__ __launch_bounds__(256) void reduce_ln_kernel(const float* __restrict__ slabs, int nslab, size_t slab_stride, const bf16_t* __restrict__ bias, const bf16_t* __restrict__ xi,
                                                        bf16_t* __restrict__ xo, bf16_t* __restrict__ xn) {
    const int row = blockIdx.x * 4 + (threadIdx.x >> 6), lane = threadIdx.x & 63;
    float acc[16];
#pragma unroll
    for (int j = 0; j < 16; ++j) acc[j] = 0.f;
    for (int sidx = 0; sidx < nslab; ++sidx) {
        const float* p = slabs + (size_t)sidx * slab_stride + (size_t)row * DM;
#pragma unroll
        for (int hseg = 0; hseg < 2; ++hseg) {
            f32x4 a = *(const f32x4*)(p + hseg * 512 + lane * 8), b = *(const f32x4*)(p + hseg * 512 + lane * 8 + 4);
#pragma unroll
            for (int j = 0; j < 4; ++j) { acc[hseg * 8 + j] += a[j]; acc[hseg * 8 + 4 + j] += b[j]; }
        }
    }
    float xv[16], bv[16];
    unpack8(*(const u32x4*)(xi + (size_t)row * DM + lane * 8), xv); unpack8(*(const u32x4*)(xi + (size_t)row * DM + 512 + lane * 8), xv + 8);
    if (bias) { unpack8(*(const u32x4*)(bias + lane * 8), bv); unpack8(*(const u32x4*)(bias + 512 + lane * 8), bv + 8); }
#pragma unroll
    for (int j = 0; j < 16; ++j) { float t = rbf(acc[j]); if (bias) t = rbf(t + bv[j]); xv[j] = rbf(xv[j] + t); }
    u32x4 a, b;
    a.x = cvt_pk_bf16(xv[0], xv[1]); a.y = cvt_pk_bf16(xv[2], xv[3]); a.z = cvt_pk_bf16(xv[4], xv[5]); a.w = cvt_pk_bf16(xv[6], xv[7]);
    b.x = cvt_pk_bf16(xv[8], xv[9]); b.y = cvt_pk_bf16(xv[10], xv[11]); b.z = cvt_pk_bf16(xv[12], xv[13]); b.w = cvt_pk_bf16(xv[14], xv[15]);
    *(u32x4*)(xo + (size_t)row * DM + lane * 8) = a; *(u32x4*)(xo + (size_t)row * DM + 512 + lane * 8) = b;
    if (xn) ln16(xv, xn + (size_t)row * DM, lane);
}

__global__ __launch_bounds__(256) void attn_kernel(const bf16_t* __restrict__ qkv, const bf16_t* __restrict__ kmem, const bf16_t* __restrict__ vmem, bf16_t* __restrict__ o) {
    __shared__ __attribute__((aligned(16))) unsigned char shm[4 * 64 * 272];
    const int w = threadIdx.x >> 6, lane = threadIdx.x & 63;
    const int task = blockIdx.x * 4 + w, seg = task >> 5, h = task & 31;
    const int fr = lane & 15, g4 = lane >> 4;
    LAS unsigned char* vl = (LAS unsigned char*)shm + w * 64 * 272;
    const bf16_t* qb = qkv + (size_t)(seg * 32) * 12288 + h * 384;
    const bf16_t* km = kmem + (size_t)(seg * 32 + h) * 1024;
    const bf16_t* vm = vmem + (size_t)(seg * 32 + h) * 1024;
#pragma unroll
    for (int i = 0; i < 16; ++i) { const int key = i * 4 + g4, ch = fr;
        u32x4 v = {0u, 0u, 0u, 0u};
        if (key < 8) v = *(const u32x4*)(vm + key * 128 + ch * 8);
        else if (key < 40) v = *(const u32x4*)(qb + (size_t)(key - 8) * 12288 + 256 + ch * 8);
        *(LAS u32x4*)(vl + key * 272 + ch * 16) = v; }
    f32x4 sc[3][2];
#pragma unroll
    for (int kt = 0; kt < 3; ++kt)
#pragma unroll
        for (int qt = 0; qt < 2; ++qt) sc[kt][qt] = (f32x4){0.f, 0.f, 0.f, 0.f};
    const bf16_t* krow[3];
#pragma unroll
    for (int kt = 0; kt < 3; ++kt) { int key = kt * 16 + fr; if (key > 39) key = 39; krow[kt] = key < 8 ? km + key * 128 : qb + (size_t)(key - 8) * 12288 + 128; }
#pragma unroll
    for (int ks = 0; ks < 4; ++ks) {
        bf16x8 qf[2];
#pragma unroll
        for (int qt = 0; qt < 2; ++qt) qf[qt] = *(const bf16x8*)(qb + (size_t)(qt * 16 + fr) * 12288 + ks * 32 + g4 * 8);
#pragma unroll
        for (int kt = 0; kt < 3; ++kt) { bf16x8 kf = *(const bf16x8*)(krow[kt] + ks * 32 + g4 * 8);
#pragma unroll
            for (int qt = 0; qt < 2; ++qt) sc[kt][qt] = __builtin_amdgcn_mfma_f32_16x16x32_bf16(kf, qf[qt], sc[kt][qt], 0, 0, 0); }
    }
    unsigned pk[2][3][2];
#pragma unroll
    for (int qt = 0; qt < 2; ++qt) {
        const int qi = qt * 16 + fr; float sv[3][4]; float m = -3.0e38f;
#pragma unroll
        for (int kt = 0; kt < 3; ++kt)
#pragma unroll
            for (int j = 0; j < 4; ++j) { const int key = kt * 16 + 4 * g4 + j; float v = rbf(rbf(sc[kt][qt][j]) * 0.08838834764831845f);
                const bool ok = key < 8 || (key < 40 && key - 8 <= qi); v = ok ? v : -1.0e9f; sv[kt][j] = v; m = fmaxf(m, v); }
        m = fmaxf(m, __shfl_xor(m, 16)); m = fmaxf(m, __shfl_xor(m, 32));
        float d = 0.f;
#pragma unroll
        for (int kt = 0; kt < 3; ++kt)
#pragma unroll
            for (int j = 0; j < 4; ++j) { const int key = kt * 16 + 4 * g4 + j; float e = key < 40 ? __expf(sv[kt][j] - m) : 0.f; sv[kt][j] = e; d += e; }
        d += __shfl_xor(d, 16); d += __shfl_xor(d, 32);
        const float inv = 1.0f / d;
#pragma unroll
        for (int kt = 0; kt < 3; ++kt) { pk[qt][kt][0] = cvt_pk_bf16(sv[kt][0] * inv, sv[kt][1] * inv); pk[qt][kt][1] = cvt_pk_bf16(sv[kt][2] * inv, sv[kt][3] * inv); }
    }
    __builtin_amdgcn_s_waitcnt(0xc07f);
    const int q4 = (lane & 15) >> 2, p4 = lane & 3;
#pragma unroll
    for (int et = 0; et < 8; ++et) {
        f32x4 oa[2] = {{0.f, 0.f, 0.f, 0.f}, {0.f, 0.f, 0.f, 0.f}};
#pragma unroll
        for (int kst = 0; kst < 2; ++kst) {
            LAS unsigned char* a0 = vl + ((2 * kst) * 16 + 4 * g4 + q4) * 272 + (16 * et + 4 * p4) * 2;
            v4s lo = __builtin_amdgcn_ds_read_tr16_b64_v4i16((LAS v4s*)a0);
            v4s hi = __builtin_amdgcn_ds_read_tr16_b64_v4i16((LAS v4s*)(a0 + 16 * 272));
            bf16x8 vf; vf[0] = lo[0]; vf[1] = lo[1]; vf[2] = lo[2]; vf[3] = lo[3]; vf[4] = hi[0]; vf[5] = hi[1]; vf[6] = hi[2]; vf[7] = hi[3];
#pragma unroll
            for (int qt = 0; qt < 2; ++qt) {
                u32x4 pw; pw.x = pk[qt][2 * kst][0]; pw.y = pk[qt][2 * kst][1];
                if (kst == 0) { pw.z = pk[qt][1][0]; pw.w = pk[qt][1][1]; } else { pw.z = 0u; pw.w = 0u; }
                oa[qt] = __builtin_amdgcn_mfma_f32_16x16x32_bf16(vf, __builtin_bit_cast(bf16x8, pw), oa[qt], 0, 0, 0);
            }
        }
#pragma unroll
        for (int qt = 0; qt < 2; ++qt) { u32x2 ov; ov.x = cvt_pk_bf16(oa[qt][0], oa[qt][1]); ov.y = cvt_pk_bf16(oa[qt][2], oa[qt][3]);
            *(u32x2*)(o + (size_t)(seg * 32 + qt * 16 + fr) * 4096 + h * 128 + et * 16 + 4 * g4) = ov; }
    }
}

extern "C" void kernel_launch(void* const* d_in, const int* in_sizes, int n_in, void* d_out, int out_size, void* d_ws, size_t ws_size, hipStream_t stream) {
    const bf16_t* x = (const bf16_t*)d_in[0]; const bf16_t* mem0 = (const bf16_t*)d_in[1];
    const bf16_t* Wq = (const bf16_t*)d_in[2]; const bf16_t* Wk = (const bf16_t*)d_in[3]; const bf16_t* Wv = (const bf16_t*)d_in[4]; const bf16_t* Wo = (const bf16_t*)d_in[5];
    const bf16_t* w1 = (const bf16_t*)d_in[8]; const bf16_t* b1 = (const bf16_t*)d_in[9]; const bf16_t* w2 = (const bf16_t*)d_in[10]; const bf16_t* b2 = (const bf16_t*)d_in[11];
    bf16_t* out = (bf16_t*)d_out;
    char* ws = (char*)d_ws; size_t off = 0;
    auto alloc = [&](size_t bytes) { char* p = ws + off; off += (bytes + 255) & ~(size_t)255; return p; };
    unsigned* membuf = (unsigned*)alloc(64 * 32768); unsigned* Ubuf = (unsigned*)alloc(64 * 32768); unsigned* Xbuf = (unsigned*)alloc(64 * 8192);
    const size_t sent_bytes = off;
    unsigned* tmo = (unsigned*)alloc(256);
    bf16_t* kvmem = (bf16_t*)alloc((size_t)4 * 64 * 32 * 1024 * 2); bf16_t* kmem = kvmem; bf16_t* vmem = kvmem + (size_t)64 * 32 * 1024;
    bf16_t* Wqkv_t = (bf16_t*)alloc((size_t)12288 * 1024 * 2); bf16_t* Wo_t = (bf16_t*)alloc((size_t)1024 * 4096 * 2);
    bf16_t* w1_t = (bf16_t*)alloc((size_t)FF * 1024 * 2); bf16_t* w2_t = (bf16_t*)alloc((size_t)1024 * FF * 2);
    bf16_t* xn = (bf16_t*)alloc((size_t)S_ * DM * 2); bf16_t* xa = (bf16_t*)alloc((size_t)S_ * DM * 2); bf16_t* xb = (bf16_t*)alloc((size_t)S_ * DM * 2);
    bf16_t* qkv = (bf16_t*)alloc((size_t)S_ * 12288 * 2); bf16_t* ob = (bf16_t*)alloc((size_t)S_ * 4096 * 2); bf16_t* hb = (bf16_t*)alloc((size_t)S_ * FF * 2);
    float* slabs = (float*)alloc((size_t)8 * S_ * DM * 4);
    const size_t slab_stride = (size_t)S_ * DM;

    hipMemsetAsync(tmo, 0, 256, stream);
    ChainArgs ca; ca.mem0 = mem0; ca.Wq = Wq; ca.Wk = Wk; ca.Wv = Wv; ca.Wo = Wo; ca.qkv = qkv; ca.qstride = 0; ca.pad0 = 0; ca.membuf = membuf; ca.Ubuf = Ubuf; ca.Xbuf = Xbuf; ca.kvmem = kvmem; ca.tmo = tmo; ca.lag = 1000; ca.nstep = 64;
    hipFuncSetAttribute((const void*)chain_kernel, hipFuncAttributeMaxDynamicSharedMemorySize, CH_LDS);

    transpose_kernel<<<16 * 64, 256, 0, stream>>>(Wq, Wqkv_t, 1024, 4096, 0);
    transpose_kernel<<<16 * 64, 256, 0, stream>>>(Wk, Wqkv_t, 1024, 4096, 1);
    transpose_kernel<<<16 * 64, 256, 0, stream>>>(Wv, Wqkv_t, 1024, 4096, 2);
    transpose_kernel<<<64 * 16, 256, 0, stream>>>(Wo, Wo_t, 4096, 1024, -1);
    transpose_kernel<<<16 * 56, 256, 0, stream>>>(w1, w1_t, 1024, FF, -1);
    transpose_kernel<<<56 * 16, 256, 0, stream>>>(w2, w2_t, FF, 1024, -1);

    const int L4 = 128 * 1024, L2 = 96 * 1024;
    hipFuncSetAttribute((const void*)gemm_kernel<4, 0>, hipFuncAttributeMaxDynamicSharedMemorySize, L4);
    hipFuncSetAttribute((const void*)gemm_kernel<4, 2>, hipFuncAttributeMaxDynamicSharedMemorySize, L4);
    hipFuncSetAttribute((const void*)gemm_kernel<2, 1>, hipFuncAttributeMaxDynamicSharedMemorySize, L2);

    ln_kernel<<<S_ / 4, 256, 0, stream>>>(x, xn);
    const bf16_t* xin = x;
    for (int layer = 0; layer < 2; ++layer) {
        gemm_kernel<4, 0><<<8 * 48, 512, L4, stream>>>(xn, 1024, Wqkv_t, 1024, qkv, 12288, nullptr, 8, 48, 16, 0);
        hipMemsetAsync(ws, 0xFF, sent_bytes, stream);
        chain_kernel<<<64, 512, CH_LDS, stream>>>(ca);
        attn_kernel<<<512, 256, 0, stream>>>(qkv, kmem, vmem, ob);
        gemm_kernel<4, 2><<<32 * 8, 512, L4, stream>>>(ob, 4096, Wo_t, 4096, slabs, 1024, nullptr, 8, 4, 8, slab_stride);
        reduce_ln_kernel<<<S_ / 4, 256, 0, stream>>>(slabs, 8, slab_stride, nullptr, xin, xa, xn);
        gemm_kernel<2, 1><<<16 * 14, 512, L2, stream>>>(xn, 1024, w1_t, 1024, hb, FF, b1, 16, 14, 16, 0);
        gemm_kernel<4, 2><<<32 * 7, 512, L4, stream>>>(hb, FF, w2_t, FF, slabs, 1024, nullptr, 8, 4, 8, slab_stride);
        reduce_ln_kernel<<<S_ / 4, 256, 0, stream>>>(slabs, 7, slab_stride, b2, xa, layer == 0 ? xb : out, layer == 0 ? xn : nullptr);
        xin = xb;
    }
}
```

```cpp
#include <hip/hip_runtime.h>
#include <stdint.h>
#define LAS __attribute__((address_space(3)))
#define GAS __attribute__((address_space(1)))
typedef unsigned short bf16_t;
typedef short bf16x8 __attribute__((ext_vector_type(8)));
typedef short bf16x4 __attribute__((ext_vector_type(4)));
typedef float f32x4 __attribute__((ext_vector_type(4)));
typedef float f32x2 __attribute__((ext_vector_type(2)));
typedef unsigned u32x4 __attribute__((ext_vector_type(4)));
typedef unsigned u32x2 __attribute__((ext_vector_type(2)));
typedef __bf16 bf16v2 __attribute__((ext_vector_type(2)));

__device__ __forceinline__ unsigned cvt_pk_bf16(float lo, float hi) { f32x2 v = {lo, hi}; bf16v2 r = __builtin_convertvector(v, bf16v2); return __builtin_bit_cast(unsigned, r); }
__device__ __forceinline__ float rbf(float x) { return __uint_as_float(cvt_pk_bf16(x, x) << 16); }
__device__ __forceinline__ float bflo(unsigned w) { return __uint_as_float(w << 16); }
__device__ __forceinline__ float bfhi(unsigned w) { return __uint_as_float(w & 0xFFFF0000u); }

constexpr int S_ = 2048, DM = 1024, NH = 32, DH = 128, NSEG = 64, FF = 3584;
constexpr unsigned SENT = 0xFFFFFFFFu;

struct ChainArgs {
    const bf16_t* mem0; const bf16_t* Wq; const bf16_t* Wk; const bf16_t* Wv; const bf16_t* Wo;
    const bf16_t* qkv;
    unsigned* membuf;
    unsigned* Ubuf;
    unsigned* Xbuf;
    bf16_t* kvmem;
    unsigned* tmo;
    unsigned* ctr;
    int lag; int nstep;
    unsigned qstride; unsigned pad0;
};
#define STAMP(i) do {} while (0)
#define LBAR() do { asm volatile("s_waitcnt lgkmcnt(0)" ::: "memory"); __builtin_amdgcn_s_barrier(); asm volatile("" ::: "memory"); } while (0)
#define CH_CAP 100000u
#define WK_CAP 20000u
#define CH_WL 0
#define CH_OV (128*1024)
#define CH_LDS (160*1024)

__device__ __forceinline__ u32x4 ld16_sc1(__amdgpu_buffer_rsrc_t r, unsigned off) { asm volatile("" ::: "memory"); u32x4 v = __builtin_amdgcn_raw_buffer_load_b128(r, off, 0, 16); asm volatile("" ::: "memory"); return v; }
__device__ __forceinline__ void st16_sc1(u32x4 v, __amdgpu_buffer_rsrc_t r, unsigned off) { __builtin_amdgcn_raw_buffer_store_b128(v, r, off, 0, 16); }
__device__ __forceinline__ void st8b_sc1(u32x2 v, __amdgpu_buffer_rsrc_t r, unsigned off) { __builtin_amdgcn_raw_buffer_store_b64(v, r, off, 0, 16); }
__device__ __forceinline__ void st8_sc1(void* p, u32x2 v) { __hip_atomic_store((GAS unsigned long long*)p, ((unsigned long long)v.y << 32) | v.x, __ATOMIC_RELAXED, __HIP_MEMORY_SCOPE_AGENT); }
__device__ __forceinline__ void st4_sc1(unsigned* p, unsigned v) { __hip_atomic_store((GAS unsigned*)p, v, __ATOMIC_RELAXED, __HIP_MEMORY_SCOPE_AGENT); }

typedef short v4s __attribute__((ext_vector_type(4)));
__device__ __forceinline__ bf16x8 tr_frag_at(LAS unsigned char* a0) {
    v4s lo = __builtin_amdgcn_ds_read_tr16_b64_v4i16((LAS v4s*)a0);
    v4s hi = __builtin_amdgcn_ds_read_tr16_b64_v4i16((LAS v4s*)(a0 + 4 * 256));
    bf16x8 r; r[0] = lo[0]; r[1] = lo[1]; r[2] = lo[2]; r[3] = lo[3]; r[4] = hi[0]; r[5] = hi[1]; r[6] = hi[2]; r[7] = hi[3];
    return r;
}
template <int CTRL> __device__ __forceinline__ float dpp_f(float x) { return __int_as_float(__builtin_amdgcn_update_dpp(0, __float_as_int(x), CTRL, 0xF, 0xF, false)); }
__device__ __forceinline__ float row16_max(float x) { x = fmaxf(x, dpp_f<0x128>(x)); x = fmaxf(x, dpp_f<0x124>(x)); x = fmaxf(x, dpp_f<0x122>(x)); x = fmaxf(x, dpp_f<0x121>(x)); return x; }
__device__ __forceinline__ float row16_sum(float x) { x += dpp_f<0x128>(x); x += dpp_f<0x124>(x); x += dpp_f<0x122>(x); x += dpp_f<0x121>(x); return x; }

__device__ __forceinline__ void chain_role(const ChainArgs& a, LAS unsigned char* lds, int c) {
    const int tid = threadIdx.x, w = __builtin_amdgcn_readfirstlane(tid >> 6);
    const int h = c >> 1, hf = c & 1, kh = w >> 2;
    bf16x8 wqk[32], wv[16];
    {
        const int lane = tid & 63;
        if (w == 0) {
            const int tok = lane & 15, g4 = lane >> 4;
            u32x2 o = *(const u32x2*)(a.mem0 + (tok & 7) * 1024 + c * 16 + g4 * 4);
            if (o.x == SENT) o.x = 0x7FC07FC0u; if (o.y == SENT) o.y = 0x7FC07FC0u;
            st8_sc1((void*)(a.membuf + (c >> 1) * 256 + (2 * (c & 1) + (g4 >> 1)) * 64 + tok * 4 + (g4 & 1) * 2), o);
        }
        const int g = lane >> 4, q = (lane & 15) >> 2, p = lane & 3;
        const int row = tid >> 4, ch = tid & 15;
#pragma unroll
        for (int grp = 0; grp < 2; ++grp) {
            const bf16_t* src = (ch < 8 ? a.Wq : a.Wk) + (size_t)(grp * 512 + row) * 4096 + h * 128 + hf * 64 + (ch & 7) * 8;
#pragma unroll
            for (int i = 0; i < 16; ++i)
                __builtin_amdgcn_global_load_lds((const unsigned*)(src + (size_t)i * 32 * 4096), (LAS unsigned*)(lds + i * 8192 + w * 1024), 16, 0, 0);
            asm volatile("s_waitcnt vmcnt(0)" ::: "memory");
            __syncthreads();
            LAS unsigned char* a0 = lds + (8 * g + q) * 256 + ((p < 2 ? 8 * w + 4 * p : 64 + 8 * w + 4 * (p - 2))) * 2;
            if (grp == kh) {
#pragma unroll
                for (int i = 0; i < 16; ++i) wqk[i] = tr_frag_at(a0 + i * 8192);
            } else {
#pragma unroll
                for (int i = 0; i < 16; ++i) wqk[16 + i] = tr_frag_at(a0 + i * 8192);
            }
            asm volatile("s_waitcnt lgkmcnt(0)" ::: "memory");
            __syncthreads();
        }
        {
            const bf16_t* src = a.Wv + (size_t)((ch >> 3) * 512 + row) * 4096 + h * 128 + hf * 64 + (ch & 7) * 8;
#pragma unroll
            for (int i = 0; i < 16; ++i)
                __builtin_amdgcn_global_load_lds((const unsigned*)(src + (size_t)i * 32 * 4096), (LAS unsigned*)(lds + i * 8192 + w * 1024), 16, 0, 0);
            asm volatile("s_waitcnt vmcnt(0)" ::: "memory");
            __syncthreads();
            LAS unsigned char* a0 = lds + (8 * g + q) * 256 + (64 * kh + 16 * (w & 3) + 4 * p) * 2;
#pragma unroll
            for (int i = 0; i < 16; ++i) wv[i] = tr_frag_at(a0 + i * 8192);
            asm volatile("s_waitcnt lgkmcnt(0)" ::: "memory");
            __syncthreads();
        }
#pragma unroll 1
        for (int cg = 0; cg < 4; ++cg) {
            const bf16_t* src = a.Wo + (size_t)(h * 128 + row) * 1024 + hf * 512 + cg * 128 + ch * 8;
#pragma unroll
            for (int ks = 0; ks < 4; ++ks)
                __builtin_amdgcn_global_load_lds((const unsigned*)(src + (size_t)ks * 32 * 1024), (LAS unsigned*)(lds + CH_OV + ks * 8192 + w * 1024), 16, 0, 0);
            asm volatile("s_waitcnt vmcnt(0)" ::: "memory");
            __syncthreads();
            LAS unsigned char* a0 = lds + CH_OV + (8 * g + q) * 256 + (16 * w + 4 * p) * 2;
#pragma unroll
            for (int ks = 0; ks < 4; ++ks) { bf16x8 f = tr_frag_at(a0 + ks * 8192);
                *(LAS bf16x8*)(lds + CH_WL + w * 16384 + (4 * cg + ks) * 1024 + lane * 16) = f; }
            asm volatile("s_waitcnt lgkmcnt(0)" ::: "memory");
            __syncthreads();
        }
    }
    const int T = a.nstep, L = a.lag;
    __amdgpu_buffer_rsrc_t rs_mem = __builtin_amdgcn_make_buffer_rsrc((void*)a.membuf, 0, (T + 1) * 32768, 0x00020000);
    __amdgpu_buffer_rsrc_t rs_U = __builtin_amdgcn_make_buffer_rsrc((void*)a.Ubuf, 0, 64 * 32768, 0x00020000);
    __amdgpu_buffer_rsrc_t rs_X = __builtin_amdgcn_make_buffer_rsrc((void*)a.Xbuf, 0, 64 * 8192, 0x00020000);
    __amdgpu_buffer_rsrc_t rs_q = __builtin_amdgcn_make_buffer_rsrc((void*)a.qkv, 0, a.qstride + 2048u * 24576u, 0x00020000);
    __amdgpu_buffer_rsrc_t rs_kv = __builtin_amdgcn_make_buffer_rsrc((void*)a.kvmem, 0, 4 * 64 * 32 * 2048, 0x00020000);
    LAS unsigned char* ov = lds + CH_OV;
    bool dead = false;
#pragma unroll 1
    for (int t = 0; t < T; ++t) {
        int lane = tid & 63; asm volatile("" : "+v"(lane));
        const int tok = lane & 15, g4 = lane >> 4, slot = tok >> 3;
        const int s0 = t < 64 ? t : 63, s1 = t - L < 0 ? 0 : (t - L > 63 ? 63 : t - L);
        const bool act0 = t < 64, act1 = t >= L && t - L < 64;
        const int sseg = slot ? s1 : s0; const bool act = slot ? act1 : act0;
        const unsigned tag = t & 1;
        STAMP(0);
        if ((act0 && (s0 & 3) == 0) || (act1 && (s1 & 3) == 0)) {
            unsigned spins = 0;
            for (;;) {
                unsigned v0 = 48u, v1 = 48u;
                if (act0 && (s0 & 3) == 0) v0 = __hip_atomic_load((GAS unsigned*)(a.ctr + ((0 * 8 + 0) * 16 + (s0 >> 2)) * 64), __ATOMIC_RELAXED, __HIP_MEMORY_SCOPE_AGENT);
                if (act1 && (s1 & 3) == 0) v1 = __hip_atomic_load((GAS unsigned*)(a.ctr + ((1 * 8 + 0) * 16 + (s1 >> 2)) * 64), __ATOMIC_RELAXED, __HIP_MEMORY_SCOPE_AGENT);
                if (__builtin_amdgcn_readfirstlane(v0) >= 48u && __builtin_amdgcn_readfirstlane(v1) >= 48u) break;
                if (dead || ++spins > CH_CAP) { dead = true; if (lane == 0) st4_sc1(a.tmo, 5u); break; }
            }
            asm volatile("" ::: "memory");
        }
        u32x2 kx[2];
        const unsigned qo0 = act0 ? (unsigned)s0 * 786432u + h * 768 : 0x80000000u, qo1 = act1 ? (unsigned)s1 * 786432u + a.qstride + h * 768 : 0x80000000u;
        {   const unsigned kp = ((g4 >> 1) ? qo1 : qo0) + tok * 24576 + (128 + hf * 64 + 8 * w + 4 * (g4 & 1)) * 2;
            kx[0] = __builtin_amdgcn_raw_buffer_load_b64(rs_q, kp, 0, 16); kx[1] = __builtin_amdgcn_raw_buffer_load_b64(rs_q, kp + 16 * 24576, 0, 16); }
        {
            u32x4 v[4];
            {
                unsigned spins = 0;
                for (;;) {
                    bool ok = true;
#pragma unroll
                    for (int i = 0; i < 4; ++i) v[i] = ld16_sc1(rs_mem, t * 32768 + w * 4096 + i * 1024 + lane * 16);
#pragma unroll
                    for (int i = 0; i < 4; ++i) ok = ok && v[i].x != SENT && v[i].y != SENT && v[i].z != SENT && v[i].w != SENT;
                    if (__all(ok)) break;
                    if (dead || ++spins > CH_CAP) { dead = true; if (lane == 0) st4_sc1(a.tmo, 1u); break; }
                }
            }
#pragma unroll
            for (int i = 0; i < 4; ++i) *(LAS u32x4*)(ov + w * 4096 + i * 1024 + lane * 16) = v[i];
        }
        STAMP(1);
        LBAR();
        STAMP(2);
        f32x4 aqk = {0.f, 0.f, 0.f, 0.f}, av = {0.f, 0.f, 0.f, 0.f};
        {
            LAS unsigned char* mf = ov + lane * 16;
            const int kofs = kh * 16384;
            bf16x8 bb[3];
            bb[0] = *(LAS bf16x8*)(mf + kofs); bb[1] = *(LAS bf16x8*)(mf + kofs + 1024);
#pragma unroll
            for (int i = 0; i < 32; ++i) {
                if (i + 2 < 32) bb[(i + 2) % 3] = *(LAS bf16x8*)(mf + (((i + 2) * 1024) & 16383) + (i + 2 < 16 ? kofs : (16384 - kofs)));
                __builtin_amdgcn_sched_barrier(0);
                aqk = __builtin_amdgcn_mfma_f32_16x16x32_bf16(wqk[i], bb[i % 3], aqk, 0, 0, 0);
                if (i < 16) av = __builtin_amdgcn_mfma_f32_16x16x32_bf16(wv[i], bb[i % 3], av, 0, 0, 0);
                __builtin_amdgcn_sched_barrier(0);
            }
        }
        STAMP(3);
        u32x2 x2; x2.x = cvt_pk_bf16(aqk[0], aqk[1]); x2.y = cvt_pk_bf16(aqk[2], aqk[3]);
        const unsigned kvo = (unsigned)(((slot * 2) * 64 + sseg) * 32 + h) * 2048 + (tok & 7) * 256 + hf * 128;
        if (act && g4 >= 2) st8b_sc1(x2, rs_kv, kvo + 16 * w + 8 * (g4 - 2));
        f32x4 sm, sx0, sx1;
        {
            auto r0 = __builtin_amdgcn_permlane32_swap(x2.x, x2.x, false, false);
            auto r1 = __builtin_amdgcn_permlane32_swap(x2.y, x2.y, false, false);
            u32x2 qa, kb; qa.x = r0[0]; qa.y = r1[0]; kb.x = r0[1]; kb.y = r1[1];
            if ((g4 >> 1) != slot) { qa.x = 0u; qa.y = 0u; }
            const f32x4 z = {0.f, 0.f, 0.f, 0.f};
            sm = __builtin_amdgcn_mfma_f32_16x16x16bf16_1k(__builtin_bit_cast(bf16x4, kb), __builtin_bit_cast(bf16x4, qa), z, 0, 0, 0);
            sx0 = __builtin_amdgcn_mfma_f32_16x16x16bf16_1k(__builtin_bit_cast(bf16x4, kx[0]), __builtin_bit_cast(bf16x4, qa), z, 0, 0, 0);
            sx1 = __builtin_amdgcn_mfma_f32_16x16x16bf16_1k(__builtin_bit_cast(bf16x4, kx[1]), __builtin_bit_cast(bf16x4, qa), z, 0, 0, 0);
        }
        STAMP(4);
        LBAR();
        {
            LAS unsigned char* sp = ov + w * 2560 + tok * 160;
            if ((g4 >> 1) == slot) *(LAS f32x4*)(sp + (g4 & 1) * 16) = sm;
            *(LAS f32x4*)(sp + 32 + g4 * 16) = sx0; *(LAS f32x4*)(sp + 96 + g4 * 16) = sx1;
        }
        if (w < 4) *(LAS f32x4*)(ov + 20480 + w * 1024 + lane * 16) = av;
        LBAR();
        STAMP(5);
        const unsigned vp = (kh ? qo1 : qo0) + ((w & 3) * 8 + (lane >> 3)) * 24576 + 512 + (lane & 7) * 32;
        const u32x4 vx0 = __builtin_amdgcn_raw_buffer_load_b128(rs_q, vp, 0, 16), vx1 = __builtin_amdgcn_raw_buffer_load_b128(rs_q, vp + 16, 0, 16);
        LAS unsigned char* vxd = ov + kh * 10240 + (8 + (w & 3) * 8 + (lane >> 3)) * 256 + (lane & 7) * 32;
        if (w >= 4) {
            av += *(LAS f32x4*)(ov + 20480 + (w - 4) * 1024 + lane * 16);
            u32x2 vb; vb.x = cvt_pk_bf16(av[0], av[1]); vb.y = cvt_pk_bf16(av[2], av[3]);
            { u32x4 o; o.x = (vb.x << 16) | tag; o.y = (vb.x & 0xFFFF0000u) | tag; o.z = (vb.y << 16) | tag; o.w = (vb.y & 0xFFFF0000u) | tag;
                st16_sc1(o, rs_X, (unsigned)(c * 8192 + 4096 + (w - 4) * 1024 + lane * 16)); }
            if (act) st8b_sc1(vb, rs_kv, kvo + 64 * 32 * 2048 + 32 * (w - 4) + 8 * g4);
            STAMP(6);
            LBAR();
            *(LAS u32x2*)(ov + slot * 10240 + (tok & 7) * 256 + (hf * 64 + 16 * (w - 4) + 4 * g4) * 2) = vb;
            *(LAS u32x4*)vxd = vx0; *(LAS u32x4*)(vxd + 16) = vx1;
            STAMP(7);
            u32x4 pv; unsigned spins = 0;
            for (;;) {
                pv = ld16_sc1(rs_X, (unsigned)((c ^ 1) * 8192 + 4096 + (w - 4) * 1024 + lane * 16));
                if (__all(((pv.x ^ tag) | (pv.y ^ tag) | (pv.z ^ tag) | (pv.w ^ tag)) & 1u ? false : true)) break;
                if (dead || ++spins > CH_CAP) { dead = true; if (lane == 0) st4_sc1(a.tmo, 3u); break; }
            }
            u32x2 o; o.x = (pv.x >> 16) | (pv.y & 0xFFFF0000u); o.y = (pv.z >> 16) | (pv.w & 0xFFFF0000u);
            *(LAS u32x2*)(ov + slot * 10240 + (tok & 7) * 256 + ((hf ^ 1) * 64 + 16 * (w - 4) + 4 * g4) * 2) = o;
        } else {
            const int qt = 4 * w + g4, gr = tok < 10 ? tok : 9;
            f32x4 own = (f32x4){0.f, 0.f, 0.f, 0.f};
#pragma unroll
            for (int ww = 0; ww < 8; ++ww) own += *(LAS f32x4*)(ov + ww * 2560 + qt * 160 + gr * 16);
            u32x4 o; o.x = (__float_as_uint(own[0]) & ~1u) | tag; o.y = (__float_as_uint(own[1]) & ~1u) | tag; o.z = (__float_as_uint(own[2]) & ~1u) | tag; o.w = (__float_as_uint(own[3]) & ~1u) | tag;
            if (tok < 10) st16_sc1(o, rs_X, (unsigned)(c * 8192 + qt * 160 + tok * 16));
            STAMP(6);
            LBAR();
            *(LAS u32x4*)vxd = vx0; *(LAS u32x4*)(vxd + 16) = vx1;
            STAMP(7);
            u32x4 ps; unsigned spins = 0;
            for (;;) {
                ps = ld16_sc1(rs_X, (unsigned)((c ^ 1) * 8192 + qt * 160 + gr * 16));
                if (__all(((ps.x ^ tag) | (ps.y ^ tag) | (ps.z ^ tag) | (ps.w ^ tag)) & 1u ? false : true)) break;
                if (dead || ++spins > CH_CAP) { dead = true; if (lane == 0) st4_sc1(a.tmo, 4u); break; }
            }
            float sv[4]; float m = -3.0e38f;
#pragma unroll
            for (int j = 0; j < 4; ++j) { const float s = __uint_as_float(o[j] & ~1u) + __uint_as_float(ps[j] & ~1u); sv[j] = rbf(rbf(s) * 0.08838834764831845f); m = fmaxf(m, sv[j]); }
            if (tok >= 10) m = -3.0e38f;
            m = row16_max(m);
            float e[4], d = 0.f;
#pragma unroll
            for (int j = 0; j < 4; ++j) { e[j] = tok < 10 ? __expf(sv[j] - m) : 0.f; d += e[j]; }
            d = row16_sum(d);
            const float inv = 1.0f / d;
            u32x2 pw; pw.x = cvt_pk_bf16(e[0] * inv, e[1] * inv); pw.y = cvt_pk_bf16(e[2] * inv, e[3] * inv);
            if (tok < 12) *(LAS u32x2*)(ov + 20480 + qt * 96 + tok * 8) = pw;
        }
        STAMP(8);
        LBAR();
        STAMP(9);
        {
            const u32x4 p0 = *(LAS u32x4*)(ov + 20480 + tok * 96 + g4 * 16);
            const u32x2 p1 = *(LAS u32x2*)(ov + 20480 + tok * 96 + 64 + g4 * 8);
            f32x4 oa = {0.f, 0.f, 0.f, 0.f};
            const int q4 = (lane & 15) >> 2, p4 = lane & 3;
#pragma unroll
            for (int cc = 0; cc < 2; ++cc) {
                LAS unsigned char* a0 = ov + cc * 10240 + (8 * g4 + q4) * 256 + (16 * w + 4 * p4) * 2;
                v4s lo = __builtin_amdgcn_ds_read_tr16_b64_v4i16((LAS v4s*)a0);
                v4s hi = __builtin_amdgcn_ds_read_tr16_b64_v4i16((LAS v4s*)(a0 + 4 * 256));
                int r2 = 32 + 4 * g4 + q4; if (r2 > 39) r2 = 39;
                v4s l2 = __builtin_amdgcn_ds_read_tr16_b64_v4i16((LAS v4s*)(ov + cc * 10240 + r2 * 256 + (16 * w + 4 * p4) * 2));
                bf16x8 vf; vf[0] = lo[0]; vf[1] = lo[1]; vf[2] = lo[2]; vf[3] = lo[3]; vf[4] = hi[0]; vf[5] = hi[1]; vf[6] = hi[2]; vf[7] = hi[3];
                u32x4 pa = p0; u32x2 pb = p1; if (slot != cc) { pa = (u32x4){0u, 0u, 0u, 0u}; pb = (u32x2){0u, 0u}; }
                oa = __builtin_amdgcn_mfma_f32_16x16x32_bf16(vf, __builtin_bit_cast(bf16x8, pa), oa, 0, 0, 0);
                oa = __builtin_amdgcn_mfma_f32_16x16x16bf16_1k(l2, __builtin_bit_cast(bf16x4, pb), oa, 0, 0, 0);
            }
            u32x2 ob; ob.x = cvt_pk_bf16(oa[0], oa[1]); ob.y = cvt_pk_bf16(oa[2], oa[3]);
            *(LAS u32x2*)(ov + 24576 + (w >> 1) * 1024 + (2 * (w & 1) + (g4 >> 1)) * 256 + tok * 16 + (g4 & 1) * 8) = ob;
        }
        LAS unsigned char* wl = lds + CH_WL + w * 16384 + lane * 16;
        __builtin_amdgcn_sched_barrier(0);
        bf16x8 wa[4];
#pragma unroll
        for (int i = 0; i < 4; ++i) wa[i] = *(LAS bf16x8*)(wl + i * 1024);
        __builtin_amdgcn_sched_barrier(0);
        LBAR();
        STAMP(10);
        {
            bf16x8 of[4];
#pragma unroll
            for (int ks = 0; ks < 4; ++ks) of[ks] = *(LAS bf16x8*)(ov + 24576 + ks * 1024 + lane * 16);
            const unsigned uo = (unsigned)(((hf * 32 + w) * 32 + h) * 1024 + tok * 64 + g4 * 16);
#pragma unroll
            for (int cg = 0; cg < 4; ++cg) {
                f32x4 u = {0.f, 0.f, 0.f, 0.f};
#pragma unroll
                for (int ks = 0; ks < 4; ++ks) { u = __builtin_amdgcn_mfma_f32_16x16x32_bf16(wa[ks], of[ks], u, 0, 0, 0);
                    __builtin_amdgcn_sched_barrier(0);
                    if (cg < 3) wa[ks] = *(LAS bf16x8*)(wl + ((cg + 1) * 4 + ks) * 1024);
                    __builtin_amdgcn_sched_barrier(0); }
                u32x4 o; o.x = (__float_as_uint(u[0]) & ~1u) | tag; o.y = (__float_as_uint(u[1]) & ~1u) | tag; o.z = (__float_as_uint(u[2]) & ~1u) | tag; o.w = (__float_as_uint(u[3]) & ~1u) | tag;
                st16_sc1(o, rs_U, uo + cg * (8 * 32 * 1024));
                __builtin_amdgcn_sched_barrier(0);
            }
        }
        STAMP(11);
        {
            u32x4 uu[4]; unsigned spins = 0;
            const unsigned uoff = (unsigned)(c * 32768 + (4 * w) * 1024 + tok * 64 + g4 * 16);
            for (;;) {
#pragma unroll
                for (int i = 0; i < 4; ++i) uu[i] = ld16_sc1(rs_U, uoff + i * 1024);
                const unsigned m = tag ? 0xFFFFFFFFu : 0u; unsigned x = 0;
#pragma unroll
                for (int i = 0; i < 4; ++i) x |= (uu[i].x ^ m) | (uu[i].y ^ m) | (uu[i].z ^ m) | (uu[i].w ^ m);
                if (__all((x & 1u) == 0u)) break;
                if (dead || ++spins > CH_CAP) { dead = true; if (lane == 0) st4_sc1(a.tmo, 2u); break; }
            }
            STAMP(12);
            f32x4 r0;
#pragma unroll
            for (int j = 0; j < 4; ++j) r0[j] = (__uint_as_float(uu[0][j] & ~1u) + __uint_as_float(uu[1][j] & ~1u)) + (__uint_as_float(uu[2][j] & ~1u) + __uint_as_float(uu[3][j] & ~1u));
            LAS unsigned char* red = ov;
            *(LAS f32x4*)(red + w * 1024 + lane * 16) = r0;
            asm volatile("s_waitcnt vmcnt(0)" ::: "memory");
            LBAR();
            STAMP(13);
            if (w == 1 && lane == 0) {
                if (act0 && (s0 & 3) == 3) __hip_atomic_fetch_add((GAS unsigned*)(a.ctr + ((0 * 8 + 7) * 16 + (s0 >> 2)) * 64), 1u, __ATOMIC_RELAXED, __HIP_MEMORY_SCOPE_AGENT);
                if (act1 && (s1 & 3) == 3) __hip_atomic_fetch_add((GAS unsigned*)(a.ctr + ((1 * 8 + 7) * 16 + (s1 >> 2)) * 64), 1u, __ATOMIC_RELAXED, __HIP_MEMORY_SCOPE_AGENT);
            }
            if (w == 0) {
                f32x4 tt = {0.f, 0.f, 0.f, 0.f};
#pragma unroll
                for (int ww = 0; ww < 8; ++ww) tt += *(LAS f32x4*)(red + ww * 1024 + lane * 16);
                u32x2 o; o.x = cvt_pk_bf16(tt[0], tt[1]); o.y = cvt_pk_bf16(tt[2], tt[3]);
                if (t + 1 == L && tok >= 8) o = *(const u32x2*)(a.mem0 + (tok & 7) * 1024 + c * 16 + g4 * 4);
                if (o.x == SENT) o.x = 0x7FC07FC0u; if (o.y == SENT) o.y = 0x7FC07FC0u;
                st8b_sc1(o, rs_mem, (unsigned)((t + 1) * 32768 + (c >> 1) * 1024 + (2 * (c & 1) + (g4 >> 1)) * 256 + tok * 16 + (g4 & 1) * 8));
            }
        }
    }
}

__host__ __device__ __forceinline__ int lds_byte(int r, int c) { const int st = (r >> 4) * 2 + (c >> 5), rr = r & 15, cc = c & 31, ob = rr * 64 + cc * 2; return st * 1024 + (ob ^ (((ob >> 9) & 1) << 5)); }
__host__ __device__ __forceinline__ void stage_rc(int b, int& R, int& C) { const int st = b / 1024, sb = b % 1024, swz = sb ^ (((sb >> 9) & 1) << 5); R = (st >> 1) * 16 + swz / 64; C = (st & 1) * 32 + (swz % 64) / 2; }
__host__ __device__ __forceinline__ int perm32(int rho) { const int n = rho >> 4, i = rho & 15; return 8 * (i >> 2) + 4 * n + (i & 3); }

template <int N_> __device__ __forceinline__ void wait_v() {
    if constexpr (N_ == 0) asm volatile("s_waitcnt vmcnt(0)" ::: "memory");
    else if constexpr (N_ == 1) asm volatile("s_waitcnt vmcnt(1)" ::: "memory");
    else if constexpr (N_ == 2) asm volatile("s_waitcnt vmcnt(2)" ::: "memory");
    else if constexpr (N_ == 3) asm volatile("s_waitcnt vmcnt(3)" ::: "memory");
    else if constexpr (N_ == 4) asm volatile("s_waitcnt vmcnt(4)" ::: "memory");
    else if constexpr (N_ == 5) asm volatile("s_waitcnt vmcnt(5)" ::: "memory");
    else if constexpr (N_ == 6) asm volatile("s_waitcnt vmcnt(6)" ::: "memory");
}

template <int MT, bool PERM>
__device__ __forceinline__ void gemm_tile(LAS unsigned char* lds, const bf16_t* A, int lda, const bf16_t* Bt, int ldb, int nt, f32x4 (&acc)[2][2][MT][2]) {
    constexpr int NA = MT / 2;
    constexpr int HA = MT * 32;
    constexpr int HTA = HA * 128;
    constexpr int HTB = 16384;
    const int tid = threadIdx.x, wid = __builtin_amdgcn_readfirstlane(tid >> 6), lane = tid & 63, wr = wid >> 2, wc = wid & 3, fr = lane & 15, fq = lane >> 4;
    unsigned voffA[NA], voffB[2];
#pragma unroll
    for (int i = 0; i < NA; ++i) { int R, C; stage_rc(tid * 16 + i * 8192, R, C); voffA[i] = (unsigned)(R * lda + C) * 2u; }
#pragma unroll
    for (int i = 0; i < 2; ++i) { int R, C; stage_rc(tid * 16 + i * 8192, R, C); const int Rb = PERM ? ((R & ~31) + perm32(R & 31)) : R; voffB[i] = (unsigned)(Rb * ldb + C) * 2u; }
    const size_t kstep = 128;
    const size_t hstepA = (size_t)HA * lda * 2, hstepB = (size_t)128 * ldb * 2;
    const unsigned ldsw = (unsigned)wid * 1024u;
    const int aoff = lds_byte(wr * MT * 16 + fr, fq * 8), boff = lds_byte(wc * 32 + fr, fq * 8);
    const char* cA = (const char*)A; const char* cB = (const char*)Bt;
#define SA_(b, h) (((b) * 2 + (h)) * HTA)
#define SB_(b, h) (4 * HTA + ((b) * 2 + (h)) * HTB)
#define STAGE_A(b, h, kt) do { _Pragma("unroll") for (int _i = 0; _i < NA; ++_i) \
        __builtin_amdgcn_global_load_lds((const unsigned*)(cA + (h) * hstepA + (size_t)(kt) * kstep + voffA[_i]), (LAS unsigned*)(lds + SA_(b, h) + ldsw + _i * 8192), 16, 0, 0); } while (0)
#define STAGE_B(b, h, kt) do { _Pragma("unroll") for (int _i = 0; _i < 2; ++_i) \
        __builtin_amdgcn_global_load_lds((const unsigned*)(cB + (h) * hstepB + (size_t)(kt) * kstep + voffB[_i]), (LAS unsigned*)(lds + SB_(b, h) + ldsw + _i * 8192), 16, 0, 0); } while (0)
#define LDA_(dst, b, h) do { _Pragma("unroll") for (int m = 0; m < MT; ++m) _Pragma("unroll") for (int k = 0; k < 2; ++k) dst[m][k] = *(const LAS bf16x8*)(lds + SA_(b, h) + aoff + m * 2048 + k * 1024); } while (0)
#define LDB_(dst, b, h) do { _Pragma("unroll") for (int n = 0; n < 2; ++n) _Pragma("unroll") for (int k = 0; k < 2; ++k) dst[n][k] = *(const LAS bf16x8*)(lds + SB_(b, h) + boff + n * 2048 + k * 1024); } while (0)
#define MMA_(ai, bj, At, Bq) do { __builtin_amdgcn_s_setprio(1); _Pragma("unroll") for (int m = 0; m < MT; ++m) _Pragma("unroll") for (int n = 0; n < 2; ++n) _Pragma("unroll") for (int k = 0; k < 2; ++k) \
        acc[ai][bj][m][n] = __builtin_amdgcn_mfma_f32_16x16x32_bf16(Bq[n][k], At[m][k], acc[ai][bj][m][n], 0, 0, 0); __builtin_amdgcn_s_setprio(0); } while (0)
#define WAIT_L(n) asm volatile("s_waitcnt lgkmcnt(" #n ")" ::: "memory")
#define BAR __builtin_amdgcn_s_barrier()
#define SCHED __builtin_amdgcn_sched_barrier(0)
#pragma unroll
    for (int a = 0; a < 2; ++a)
#pragma unroll
        for (int b = 0; b < 2; ++b)
#pragma unroll
            for (int m = 0; m < MT; ++m)
#pragma unroll
                for (int n = 0; n < 2; ++n) acc[a][b][m][n] = (f32x4){0.f, 0.f, 0.f, 0.f};
    bf16x8 At[MT][2], B0[2][2], B1[2][2];
    STAGE_B(0, 0, 0); STAGE_A(0, 0, 0); STAGE_B(0, 1, 0); STAGE_A(0, 1, 0);
    if (wr == 1) BAR;
    wait_v<2 + NA>(); BAR;
    STAGE_B(1, 0, 1); STAGE_A(1, 0, 1); STAGE_B(1, 1, 1);
    wait_v<4 + NA>(); BAR;
    for (int t = 0; t < nt - 2; t += 2) {
        LDB_(B0, 0, 0); SCHED; LDA_(At, 0, 0); STAGE_A(1, 1, t + 1);
        if constexpr (MT == 4) WAIT_L(8); else WAIT_L(4);
        BAR; WAIT_L(0); MMA_(0, 0, At, B0); BAR; SCHED;
        LDB_(B1, 0, 1); STAGE_B(0, 0, t + 2);
        BAR; WAIT_L(0); MMA_(0, 1, At, B1); BAR;
        LDA_(At, 0, 1); STAGE_A(0, 0, t + 2);
        BAR; WAIT_L(0); MMA_(1, 0, At, B0); BAR; SCHED;
        STAGE_B(0, 1, t + 2);
        wait_v<4 + NA>(); BAR; MMA_(1, 1, At, B1); BAR;
        LDB_(B0, 1, 0); SCHED; LDA_(At, 1, 0); STAGE_A(0, 1, t + 2);
        if constexpr (MT == 4) WAIT_L(8); else WAIT_L(4);
        BAR; WAIT_L(0); MMA_(0, 0, At, B0); BAR; SCHED;
        LDB_(B1, 1, 1); STAGE_B(1, 0, t + 3);
        BAR; WAIT_L(0); MMA_(0, 1, At, B1); BAR;
        LDA_(At, 1, 1); STAGE_A(1, 0, t + 3);
        BAR; WAIT_L(0); MMA_(1, 0, At, B0); BAR; SCHED;
        STAGE_B(1, 1, t + 3);
        wait_v<4 + NA>(); BAR; MMA_(1, 1, At, B1); BAR;
    }
    { LDB_(B0, 0, 0); wait_v<0>(); LDA_(At, 0, 0); STAGE_A(1, 1, nt - 1);
      BAR; WAIT_L(0); MMA_(0, 0, At, B0); BAR;
      LDB_(B1, 0, 1); BAR; WAIT_L(0); MMA_(0, 1, At, B1); BAR;
      LDA_(At, 0, 1); BAR; WAIT_L(0); MMA_(1, 0, At, B0); MMA_(1, 1, At, B1); BAR; }
    { LDB_(B0, 1, 0); LDA_(At, 1, 0); BAR; WAIT_L(0); MMA_(0, 0, At, B0); BAR;
      LDB_(B1, 1, 1); wait_v<0>(); BAR; WAIT_L(0); MMA_(0, 1, At, B1); BAR;
      LDA_(At, 1, 1); BAR; WAIT_L(0); MMA_(1, 0, At, B0); MMA_(1, 1, At, B1); BAR; }
    if (wr == 0) BAR;
#undef SA_
#undef SB_
#undef STAGE_A
#undef STAGE_B
#undef LDA_
#undef LDB_
#undef MMA_
}

__global__ __launch_bounds__(256) void transpose_kernel(const bf16_t* __restrict__ W, bf16_t* __restrict__ Wt, int K, int N, int which) {
    __shared__ bf16_t tile[64][72];
    const int nb = N / 64, bk = blockIdx.x / nb, bn = blockIdx.x % nb, t = threadIdx.x;
#pragma unroll
    for (int i = 0; i < 2; ++i) { const int r = (t >> 3) + i * 32, cc = (t & 7) * 8;
        u32x4 v = *(const u32x4*)(W + (size_t)(bk * 64 + r) * N + bn * 64 + cc);
        *(u32x4*)&tile[r][cc] = v; }
    __syncthreads();
#pragma unroll
    for (int i = 0; i < 2; ++i) { const int n = (t >> 3) + i * 32, kc = (t & 7) * 8;
        unsigned short e[8];
#pragma unroll
        for (int j = 0; j < 8; ++j) e[j] = tile[kc + j][n];
        u32x4 v; v.x = e[0] | ((unsigned)e[1] << 16); v.y = e[2] | ((unsigned)e[3] << 16); v.z = e[4] | ((unsigned)e[5] << 16); v.w = e[6] | ((unsigned)e[7] << 16);
        int ng = bn * 64 + n; if (which >= 0) ng = (ng >> 7) * 384 + which * 128 + (ng & 127);
        *(u32x4*)(Wt + (size_t)ng * K + bk * 64 + kc) = v; }
}

__device__ __forceinline__ float wave_sum(float v) {
#pragma unroll
    for (int o = 32; o >= 1; o >>= 1) v += __shfl_xor(v, o);
    return v;
}
__device__ __forceinline__ void ln16(const float (&x)[16], __amdgpu_buffer_rsrc_t rs, unsigned dsto, int lane) {
    float s = 0.f;
#pragma unroll
    for (int j = 0; j < 16; ++j) s += x[j];
    const float mean = wave_sum(s) * (1.0f / 1024.0f);
    float q = 0.f;
#pragma unroll
    for (int j = 0; j < 16; ++j) { const float d = x[j] - mean; q += d * d; }
    const float var = rbf(wave_sum(q) * (1.0f / 1024.0f));
    const float mb = rbf(mean);
    const float sd = rbf(sqrtf(rbf(var + 1e-5f)));
    const float inv = 1.0f / sd;
    float y[16];
#pragma unroll
    for (int j = 0; j < 16; ++j) y[j] = rbf(x[j] - mb) * inv;
    u32x4 a, b;
    a.x = cvt_pk_bf16(y[0], y[1]); a.y = cvt_pk_bf16(y[2], y[3]); a.z = cvt_pk_bf16(y[4], y[5]); a.w = cvt_pk_bf16(y[6], y[7]);
    b.x = cvt_pk_bf16(y[8], y[9]); b.y = cvt_pk_bf16(y[10], y[11]); b.z = cvt_pk_bf16(y[12], y[13]); b.w = cvt_pk_bf16(y[14], y[15]);
    st16_sc1(a, rs, dsto + lane * 16); st16_sc1(b, rs, dsto + 1024 + lane * 16);
}
__device__ __forceinline__ void unpack8(u32x4 v, float* x) { x[0] = bflo(v.x); x[1] = bfhi(v.x); x[2] = bflo(v.y); x[3] = bfhi(v.y); x[4] = bflo(v.z); x[5] = bfhi(v.z); x[6] = bflo(v.w); x[7] = bfhi(v.w); }

__device__ __forceinline__ void attn_task(LAS unsigned char* lds, int seg, int h, const bf16_t* qkv, const bf16_t* kmem, const bf16_t* vmem, __amdgpu_buffer_rsrc_t rs, unsigned ob_off) {
    const int w = threadIdx.x >> 6, lane = threadIdx.x & 63;
    const int fr = lane & 15, g4 = lane >> 4;
    LAS unsigned char* vl = lds + w * 64 * 272;
    const bf16_t* qb = qkv + (size_t)(seg * 32) * 12288 + h * 384;
    const bf16_t* km = kmem + (size_t)(seg * 32 + h) * 1024;
    const bf16_t* vm = vmem + (size_t)(seg * 32 + h) * 1024;
#pragma unroll
    for (int i = 0; i < 16; ++i) { const int key = i * 4 + g4, ch = fr;
        u32x4 v = {0u, 0u, 0u, 0u};
        if (key < 8) v = *(const u32x4*)(vm + key * 128 + ch * 8);
        else if (key < 40) v = *(const u32x4*)(qb + (size_t)(key - 8) * 12288 + 256 + ch * 8);
        *(LAS u32x4*)(vl + key * 272 + ch * 16) = v; }
    f32x4 sc[3][2];
#pragma unroll
    for (int kt = 0; kt < 3; ++kt)
#pragma unroll
        for (int qt = 0; qt < 2; ++qt) sc[kt][qt] = (f32x4){0.f, 0.f, 0.f, 0.f};
    const bf16_t* krow[3];
#pragma unroll
    for (int kt = 0; kt < 3; ++kt) { int key = kt * 16 + fr; if (key > 39) key = 39; krow[kt] = key < 8 ? km + key * 128 : qb + (size_t)(key - 8) * 12288 + 128; }
#pragma unroll
    for (int ks = 0; ks < 4; ++ks) {
        bf16x8 qf[2];
#pragma unroll
        for (int qt = 0; qt < 2; ++qt) qf[qt] = *(const bf16x8*)(qb + (size_t)(qt * 16 + fr) * 12288 + ks * 32 + g4 * 8);
#pragma unroll
        for (int kt = 0; kt < 3; ++kt) { bf16x8 kf = *(const bf16x8*)(krow[kt] + ks * 32 + g4 * 8);
#pragma unroll
            for (int qt = 0; qt < 2; ++qt) sc[kt][qt] = __builtin_amdgcn_mfma_f32_16x16x32_bf16(kf, qf[qt], sc[kt][qt], 0, 0, 0); }
    }
    unsigned pk[2][3][2];
#pragma unroll
    for (int qt = 0; qt < 2; ++qt) {
        const int qi = qt * 16 + fr; float sv[3][4]; float m = -3.0e38f;
#pragma unroll
        for (int kt = 0; kt < 3; ++kt)
#pragma unroll
            for (int j = 0; j < 4; ++j) { const int key = kt * 16 + 4 * g4 + j; float v = rbf(rbf(sc[kt][qt][j]) * 0.08838834764831845f);
                const bool ok = key < 8 || (key < 40 && key - 8 <= qi); v = ok ? v : -1.0e9f; sv[kt][j] = v; m = fmaxf(m, v); }
        m = fmaxf(m, __shfl_xor(m, 16)); m = fmaxf(m, __shfl_xor(m, 32));
        float d = 0.f;
#pragma unroll
        for (int kt = 0; kt < 3; ++kt)
#pragma unroll
            for (int j = 0; j < 4; ++j) { const int key = kt * 16 + 4 * g4 + j; float e = key < 40 ? __expf(sv[kt][j] - m) : 0.f; sv[kt][j] = e; d += e; }
        d += __shfl_xor(d, 16); d += __shfl_xor(d, 32);
        const float inv = 1.0f / d;
#pragma unroll
        for (int kt = 0; kt < 3; ++kt) { pk[qt][kt][0] = cvt_pk_bf16(sv[kt][0] * inv, sv[kt][1] * inv); pk[qt][kt][1] = cvt_pk_bf16(sv[kt][2] * inv, sv[kt][3] * inv); }
    }
    __builtin_amdgcn_s_waitcnt(0xc07f);
    const int q4 = (lane & 15) >> 2, p4 = lane & 3;
#pragma unroll
    for (int et = 0; et < 8; ++et) {
        f32x4 oa[2] = {{0.f, 0.f, 0.f, 0.f}, {0.f, 0.f, 0.f, 0.f}};
#pragma unroll
        for (int kst = 0; kst < 2; ++kst) {
            LAS unsigned char* a0 = vl + ((2 * kst) * 16 + 4 * g4 + q4) * 272 + (16 * et + 4 * p4) * 2;
            v4s lo = __builtin_amdgcn_ds_read_tr16_b64_v4i16((LAS v4s*)a0);
            v4s hi = __builtin_amdgcn_ds_read_tr16_b64_v4i16((LAS v4s*)(a0 + 16 * 272));
            bf16x8 vf; vf[0] = lo[0]; vf[1] = lo[1]; vf[2] = lo[2]; vf[3] = lo[3]; vf[4] = hi[0]; vf[5] = hi[1]; vf[6] = hi[2]; vf[7] = hi[3];
#pragma unroll
            for (int qt = 0; qt < 2; ++qt) {
                u32x4 pw; pw.x = pk[qt][2 * kst][0]; pw.y = pk[qt][2 * kst][1];
                if (kst == 0) { pw.z = pk[qt][1][0]; pw.w = pk[qt][1][1]; } else { pw.z = 0u; pw.w = 0u; }
                oa[qt] = __builtin_amdgcn_mfma_f32_16x16x32_bf16(vf, __builtin_bit_cast(bf16x8, pw), oa[qt], 0, 0, 0);
            }
        }
#pragma unroll
        for (int qt = 0; qt < 2; ++qt) { u32x2 ov; ov.x = cvt_pk_bf16(oa[qt][0], oa[qt][1]); ov.y = cvt_pk_bf16(oa[qt][2], oa[qt][3]);
            st8b_sc1(ov, rs, ob_off + (unsigned)((seg * 32 + qt * 16 + fr) * 4096 + h * 128 + et * 16 + 4 * g4) * 2u); }
    }
}

__device__ __forceinline__ float gelu_tanh(float x) {
    const float u = 0.7978845608028654f * (x + 0.044715f * x * x * x);
    const float e = __expf(-2.0f * u);
    return x * __builtin_amdgcn_rcpf(1.0f + e);
}

struct MegaArgs {
    ChainArgs ch;
    char* ws;
    const bf16_t* x; const bf16_t* b1; const bf16_t* b2; bf16_t* out;
    unsigned* queue;
    unsigned o_wqkv, o_wo, o_w1, o_w2, o_xn1, o_xa0, o_xna0, o_xb0, o_xnb0, o_xna1, o_qkv, o_ob, o_hb, o_slab, o_kv, o_xa1;
    int D; int pad1;
};
enum { PH_QKV = 0, PH_ATT = 1, PH_OUT = 2, PH_REDA = 3, PH_FFN1 = 4, PH_FFN2 = 5, PH_REDB = 6, PH_CH = 7 };
__device__ __forceinline__ constexpr int ph_count(int ph) { return ph == PH_QKV ? 48 : ph == PH_ATT ? 16 : ph == PH_OUT ? 32 : ph == PH_REDA ? 16 : ph == PH_FFN1 ? 14 : ph == PH_FFN2 ? 28 : 16; }

__device__ __forceinline__ void wait_ctr(unsigned* p, unsigned target, unsigned* tmo) {
    unsigned spins = 0;
    for (;;) {
        const unsigned v = __hip_atomic_load((GAS unsigned*)p, __ATOMIC_RELAXED, __HIP_MEMORY_SCOPE_AGENT);
        if (__builtin_amdgcn_readfirstlane(v) >= target) break;
        __builtin_amdgcn_s_sleep(4);
        if (++spins > WK_CAP) { st4_sc1(tmo, 9u); break; }
        if ((spins & 63u) == 0u && __builtin_amdgcn_readfirstlane(__hip_atomic_load((GAS unsigned*)tmo, __ATOMIC_RELAXED, __HIP_MEMORY_SCOPE_AGENT)) != 0u) break;
    }
}

template <int MODE>
__device__ __forceinline__ void gemm_task(LAS unsigned char* lds, const bf16_t* A, int lda, const bf16_t* Bt, int ldb, int nt, __amdgpu_buffer_rsrc_t rs, unsigned c_off, int ldc, const bf16_t* bias) {
    f32x4 acc[2][2][2][2];
    gemm_tile<2, MODE != 2>(lds, A, lda, Bt, ldb, nt, acc);
    const int tid = threadIdx.x, wid = tid >> 6, lane = tid & 63, wr = wid >> 2, wc = wid & 3, fr = lane & 15, fq = lane >> 4;
    const int row0 = wr * 32 + fr;
    if constexpr (MODE == 2) {
        const unsigned col0 = wc * 32 + 4 * fq;
#pragma unroll
        for (int ai = 0; ai < 2; ++ai)
#pragma unroll
            for (int m = 0; m < 2; ++m) { const unsigned ro = c_off + ((unsigned)(row0 + ai * 64 + m * 16) * ldc + col0) * 4u;
#pragma unroll
                for (int bj = 0; bj < 2; ++bj)
#pragma unroll
                    for (int n = 0; n < 2; ++n) st16_sc1(__builtin_bit_cast(u32x4, acc[ai][bj][m][n]), rs, ro + (bj * 128 + n * 16) * 4); }
    } else {
        const unsigned col0 = wc * 32 + 8 * fq;
        float bv[2][8];
        if constexpr (MODE == 1) {
#pragma unroll
            for (int bj = 0; bj < 2; ++bj) { u32x4 b = *(const u32x4*)(bias + col0 + bj * 128);
                bv[bj][0] = bflo(b.x); bv[bj][1] = bfhi(b.x); bv[bj][2] = bflo(b.y); bv[bj][3] = bfhi(b.y); bv[bj][4] = bflo(b.z); bv[bj][5] = bfhi(b.z); bv[bj][6] = bflo(b.w); bv[bj][7] = bfhi(b.w); }
        }
#pragma unroll
        for (int ai = 0; ai < 2; ++ai)
#pragma unroll
            for (int m = 0; m < 2; ++m) { const unsigned ro = c_off + ((unsigned)(row0 + ai * 64 + m * 16) * ldc + col0) * 2u;
#pragma unroll
                for (int bj = 0; bj < 2; ++bj) { f32x4 v0 = acc[ai][bj][m][0], v1 = acc[ai][bj][m][1];
                    if constexpr (MODE == 1) {
#pragma unroll
                        for (int j = 0; j < 4; ++j) { v0[j] = gelu_tanh(rbf(rbf(v0[j]) + bv[bj][j])); v1[j] = gelu_tanh(rbf(rbf(v1[j]) + bv[bj][4 + j])); }
                    }
                    u32x4 w; w.x = cvt_pk_bf16(v0[0], v0[1]); w.y = cvt_pk_bf16(v0[2], v0[3]); w.z = cvt_pk_bf16(v1[0], v1[1]); w.w = cvt_pk_bf16(v1[2], v1[3]);
                    st16_sc1(w, rs, ro + bj * 256); } }
    }
}

__device__ __forceinline__ void red_task(const float* slabs, int nslab, const bf16_t* bias, const bf16_t* xi, bf16_t* xo_plain, __amdgpu_buffer_rsrc_t rs, unsigned xo_off, unsigned xn_off, bool do_ln, int row) {
    const int lane = threadIdx.x & 63;
    float acc[16];
#pragma unroll
    for (int j = 0; j < 16; ++j) acc[j] = 0.f;
    for (int sidx = 0; sidx < nslab; ++sidx) {
        const float* p = slabs + (size_t)sidx * ((size_t)S_ * DM) + (size_t)row * DM;
#pragma unroll
        for (int hseg = 0; hseg < 2; ++hseg) {
            f32x4 a = *(const f32x4*)(p + hseg * 512 + lane * 8), b = *(const f32x4*)(p + hseg * 512 + lane * 8 + 4);
#pragma unroll
            for (int j = 0; j < 4; ++j) { acc[hseg * 8 + j] += a[j]; acc[hseg * 8 + 4 + j] += b[j]; }
        }
    }
    float xv[16], bv[16];
    unpack8(*(const u32x4*)(xi + (size_t)row * DM + lane * 8), xv); unpack8(*(const u32x4*)(xi + (size_t)row * DM + 512 + lane * 8), xv + 8);
    if (bias) { unpack8(*(const u32x4*)(bias + lane * 8), bv); unpack8(*(const u32x4*)(bias + 512 + lane * 8), bv + 8); }
#pragma unroll
    for (int j = 0; j < 16; ++j) { float t = rbf(acc[j]); if (bias) t = rbf(t + bv[j]); xv[j] = rbf(xv[j] + t); }
    u32x4 a, b;
    a.x = cvt_pk_bf16(xv[0], xv[1]); a.y = cvt_pk_bf16(xv[2], xv[3]); a.z = cvt_pk_bf16(xv[4], xv[5]); a.w = cvt_pk_bf16(xv[6], xv[7]);
    b.x = cvt_pk_bf16(xv[8], xv[9]); b.y = cvt_pk_bf16(xv[10], xv[11]); b.z = cvt_pk_bf16(xv[12], xv[13]); b.w = cvt_pk_bf16(xv[14], xv[15]);
    if (xo_plain) { *(u32x4*)(xo_plain + (size_t)row * DM + lane * 8) = a; *(u32x4*)(xo_plain + (size_t)row * DM + 512 + lane * 8) = b; }
    else { st16_sc1(a, rs, xo_off + (unsigned)row * 2048u + lane * 16); st16_sc1(b, rs, xo_off + (unsigned)row * 2048u + 1024 + lane * 16); }
    if (do_ln) ln16(xv, rs, xn_off + (unsigned)row * 2048u, lane);
}

__device__ __forceinline__ void worker_role(const MegaArgs& a, LAS unsigned char* lds) {
    const int tid = threadIdx.x, wid = __builtin_amdgcn_readfirstlane(tid >> 6);
    __amdgpu_buffer_rsrc_t rs = __builtin_amdgcn_make_buffer_rsrc((void*)a.ws, 0, 0x10000000, 0x00020000);
    LAS unsigned* sidx = (LAS unsigned*)(lds + 159 * 1024);
    unsigned* ctr = a.ch.ctr; unsigned* tmo = a.ch.tmo;
    const int D = a.D;
#pragma unroll 1
    for (;;) {
        if (wid == 0) { const unsigned old = __hip_atomic_fetch_add((GAS unsigned*)a.queue, (tid & 63) == 0 ? 1u : 0u, __ATOMIC_RELAXED, __HIP_MEMORY_SCOPE_AGENT);
            *sidx = __builtin_amdgcn_readfirstlane(old); }
        __syncthreads();
        int idx = __builtin_amdgcn_readfirstlane(*sidx);
        __syncthreads();
        int ph = -1, l = 0, b = 0, sub = 0;
        if (idx < 768) { ph = PH_QKV; l = 0; b = idx / 48; sub = idx % 48; }
        else {
            idx -= 768;
            for (int p = 1; p <= 15 + D && ph < 0; ++p) {
                const int b0 = p - 1, b1 = p - D, bq = p - 2;
                const bool v0 = b0 >= 0 && b0 < 16, v1 = b1 >= 0 && b1 < 16, vq = bq >= 0 && bq < 16;
#define TRY(valid, PH, L_, B_) if (ph < 0 && (valid)) { if (idx < ph_count(PH)) { ph = PH; l = L_; b = B_; sub = idx; } else idx -= ph_count(PH); }
                TRY(v0, PH_ATT, 0, b0) TRY(v1, PH_ATT, 1, b1) TRY(vq, PH_QKV, 1, bq) TRY(v0, PH_OUT, 0, b0) TRY(v1, PH_OUT, 1, b1)
                TRY(v0, PH_REDA, 0, b0) TRY(v1, PH_REDA, 1, b1) TRY(v0, PH_FFN1, 0, b0) TRY(v1, PH_FFN1, 1, b1)
                TRY(v0, PH_FFN2, 0, b0) TRY(v1, PH_FFN2, 1, b1) TRY(v0, PH_REDB, 0, b0) TRY(v1, PH_REDB, 1, b1)
#undef TRY
            }
            if (ph < 0) break;
        }
        if (wid == 0) {
#define CTR(L_, PH, B_) (ctr + (((L_) * 8 + (PH)) * 16 + (B_)) * 64)
            if (ph == PH_QKV) { if (l == 1) wait_ctr(CTR(0, PH_REDB, b), 16, tmo); }
            else if (ph == PH_ATT) { wait_ctr(CTR(l, PH_QKV, b), 48, tmo); wait_ctr(CTR(l, PH_CH, b), 64, tmo); }
            else wait_ctr(CTR(l, ph - 1, b), ph_count(ph - 1), tmo);
            __builtin_amdgcn_fence(__ATOMIC_ACQUIRE, "agent");
            asm volatile("s_waitcnt vmcnt(0)" ::: "memory");
        }
        __syncthreads();
        const char* ws = a.ws;
        if (ph == PH_QKV) {
            const bf16_t* A = (const bf16_t*)(ws + (l ? a.o_xnb0 : a.o_xn1)) + (size_t)b * 128 * 1024;
            gemm_task<0>(lds, A, 1024, (const bf16_t*)(ws + a.o_wqkv) + (size_t)sub * 256 * 1024, 1024, 16, rs, a.o_qkv + ((unsigned)b * 128u * 12288u + sub * 256u) * 2u, 12288, nullptr);
        } else if (ph == PH_ATT) {
            const int task = sub * 8 + wid, seg = b * 4 + (task >> 5), h = task & 31;
            const bf16_t* km = (const bf16_t*)(ws + a.o_kv) + (size_t)(l * 2) * 64 * 32 * 1024;
            attn_task(lds, seg, h, (const bf16_t*)(ws + a.o_qkv), km, km + (size_t)64 * 32 * 1024, rs, a.o_ob);
        } else if (ph == PH_OUT) {
            const int pn = sub & 3, ks = sub >> 2;
            gemm_task<2>(lds, (const bf16_t*)(ws + a.o_ob) + (size_t)b * 128 * 4096 + ks * 512, 4096, (const bf16_t*)(ws + a.o_wo) + (size_t)pn * 256 * 4096 + ks * 512, 4096, 8, rs,
                         a.o_slab + ((unsigned)ks * (S_ * DM) + (unsigned)b * 128u * 1024u + pn * 256u) * 4u, 1024, nullptr);
        } else if (ph == PH_REDA) {
            const int row = b * 128 + sub * 8 + wid;
            red_task((const float*)(ws + a.o_slab), 8, nullptr, l ? (const bf16_t*)(ws + a.o_xb0) : a.x, nullptr, rs, l ? a.o_xa1 : a.o_xa0, l ? a.o_xna1 : a.o_xna0, true, row);
        } else if (ph == PH_FFN1) {
            gemm_task<1>(lds, (const bf16_t*)(ws + (l ? a.o_xna1 : a.o_xna0)) + (size_t)b * 128 * 1024, 1024, (const bf16_t*)(ws + a.o_w1) + (size_t)sub * 256 * 1024, 1024, 16, rs,
                         a.o_hb + ((unsigned)b * 128u * FF + sub * 256u) * 2u, FF, a.b1 + sub * 256);
        } else if (ph == PH_FFN2) {
            const int pn = sub & 3, ks = sub >> 2;
            gemm_task<2>(lds, (const bf16_t*)(ws + a.o_hb) + (size_t)b * 128 * FF + ks * 512, FF, (const bf16_t*)(ws + a.o_w2) + (size_t)pn * 256 * FF + ks * 512, FF, 8, rs,
                         a.o_slab + ((unsigned)ks * (S_ * DM) + (unsigned)b * 128u * 1024u + pn * 256u) * 4u, 1024, nullptr);
        } else {
            const int row = b * 128 + sub * 8 + wid;
            if (l == 0) red_task((const float*)(ws + a.o_slab), 7, a.b2, (const bf16_t*)(ws + a.o_xa0), nullptr, rs, a.o_xb0, a.o_xnb0, true, row);
            else red_task((const float*)(ws + a.o_slab), 7, a.b2, (const bf16_t*)(ws + a.o_xa1), a.out, rs, 0, 0, false, row);
        }
        asm volatile("s_waitcnt vmcnt(0)" ::: "memory");
        __syncthreads();
        if (wid == 0) __hip_atomic_fetch_add((GAS unsigned*)CTR(l, ph, b), (tid & 63) == 0 ? 1u : 0u, __ATOMIC_RELAXED, __HIP_MEMORY_SCOPE_AGENT);
#undef CTR
    }
}

__global__ __launch_bounds__(512, 2) void mega_kernel(MegaArgs a) {
    extern __shared__ __attribute__((aligned(16))) unsigned char shm[];
    LAS unsigned char* lds = (LAS unsigned char*)shm;
    if (blockIdx.x < 64) chain_role(a.ch, lds, blockIdx.x); else worker_role(a, lds);
}

__global__ __launch_bounds__(256) void ln_kernel(const bf16_t* __restrict__ x, char* ws, unsigned xn_off) {
    const int row = blockIdx.x * 4 + (threadIdx.x >> 6), lane = threadIdx.x & 63;
    __amdgpu_buffer_rsrc_t rs = __builtin_amdgcn_make_buffer_rsrc((void*)ws, 0, 0x10000000, 0x00020000);
    float v[16];
    unpack8(*(const u32x4*)(x + (size_t)row * DM + lane * 8), v); unpack8(*(const u32x4*)(x + (size_t)row * DM + 512 + lane * 8), v + 8);
    ln16(v, rs, xn_off + (unsigned)row * 2048u, lane);
}

#define LAG 12
extern "C" void kernel_launch(void* const* d_in, const int* in_sizes, int n_in, void* d_out, int out_size, void* d_ws, size_t ws_size, hipStream_t stream) {
    const bf16_t* x = (const bf16_t*)d_in[0]; const bf16_t* mem0 = (const bf16_t*)d_in[1];
    const bf16_t* Wq = (const bf16_t*)d_in[2]; const bf16_t* Wk = (const bf16_t*)d_in[3]; const bf16_t* Wv = (const bf16_t*)d_in[4]; const bf16_t* Wo = (const bf16_t*)d_in[5];
    const bf16_t* w1 = (const bf16_t*)d_in[8]; const bf16_t* b1 = (const bf16_t*)d_in[9]; const bf16_t* w2 = (const bf16_t*)d_in[10]; const bf16_t* b2 = (const bf16_t*)d_in[11];
    char* ws = (char*)d_ws; size_t off = 0;
    auto alloc = [&](size_t bytes) { size_t p = off; off += (bytes + 255) & ~(size_t)255; return p; };
    const int T = 64 + LAG;
    const size_t o_ctr = alloc(2 * 8 * 16 * 256), o_queue = alloc(256), o_tmo = alloc(256);
    const size_t zero_bytes = off;
    const size_t o_mem = alloc((size_t)(T + 1) * 32768), o_U = alloc(64 * 32768), o_X = alloc(64 * 8192);
    const size_t sent_bytes = off - zero_bytes;
    const size_t o_kv = alloc((size_t)4 * 64 * 32 * 1024 * 2);
    const size_t o_wqkv = alloc((size_t)12288 * 1024 * 2), o_wo = alloc((size_t)1024 * 4096 * 2), o_w1 = alloc((size_t)FF * 1024 * 2), o_w2 = alloc((size_t)1024 * FF * 2);
    const size_t XB = (size_t)S_ * DM * 2;
    const size_t o_xn1 = alloc(XB), o_xa0 = alloc(XB), o_xna0 = alloc(XB), o_xb0 = alloc(XB), o_xnb0 = alloc(XB), o_xna1 = alloc(XB), o_xa1 = alloc(XB);
    const size_t o_qkv = alloc((size_t)S_ * 12288 * 2), o_ob = alloc((size_t)S_ * 4096 * 2), o_hb = alloc((size_t)S_ * FF * 2);
    const size_t o_slab = alloc((size_t)8 * S_ * DM * 4);

    hipMemsetAsync(ws, 0, zero_bytes, stream);
    hipMemsetAsync(ws + zero_bytes, 0xFF, sent_bytes, stream);
    MegaArgs a;
    a.ch.mem0 = mem0; a.ch.Wq = Wq; a.ch.Wk = Wk; a.ch.Wv = Wv; a.ch.Wo = Wo; a.ch.qkv = (const bf16_t*)(ws + o_qkv);
    a.ch.membuf = (unsigned*)(ws + o_mem); a.ch.Ubuf = (unsigned*)(ws + o_U); a.ch.Xbuf = (unsigned*)(ws + o_X); a.ch.kvmem = (bf16_t*)(ws + o_kv);
    a.ch.tmo = (unsigned*)(ws + o_tmo); a.ch.ctr = (unsigned*)(ws + o_ctr); a.ch.lag = LAG; a.ch.nstep = T; a.ch.qstride = 0; a.ch.pad0 = 0;
    a.ws = ws; a.x = x; a.b1 = b1; a.b2 = b2; a.out = (bf16_t*)d_out; a.queue = (unsigned*)(ws + o_queue);
    a.o_wqkv = o_wqkv; a.o_wo = o_wo; a.o_w1 = o_w1; a.o_w2 = o_w2; a.o_xn1 = o_xn1; a.o_xa0 = o_xa0; a.o_xna0 = o_xna0; a.o_xb0 = o_xb0; a.o_xnb0 = o_xnb0; a.o_xna1 = o_xna1;
    a.o_qkv = o_qkv; a.o_ob = o_ob; a.o_hb = o_hb; a.o_slab = o_slab; a.o_kv = o_kv; a.o_xa1 = o_xa1; a.D = 1 + LAG / 4; a.pad1 = 0;

    bf16_t* Wqkv_t = (bf16_t*)(ws + o_wqkv);
    transpose_kernel<<<16 * 64, 256, 0, stream>>>(Wq, Wqkv_t, 1024, 4096, 0);
    transpose_kernel<<<16 * 64, 256, 0, stream>>>(Wk, Wqkv_t, 1024, 4096, 1);
    transpose_kernel<<<16 * 64, 256, 0, stream>>>(Wv, Wqkv_t, 1024, 4096, 2);
    transpose_kernel<<<64 * 16, 256, 0, stream>>>(Wo, (bf16_t*)(ws + o_wo), 4096, 1024, -1);
    transpose_kernel<<<16 * 56, 256, 0, stream>>>(w1, (bf16_t*)(ws + o_w1), 1024, FF, -1);
    transpose_kernel<<<56 * 16, 256, 0, stream>>>(w2, (bf16_t*)(ws + o_w2), FF, 1024, -1);
    ln_kernel<<<S_ / 4, 256, 0, stream>>>(x, ws, (unsigned)o_xn1);
    hipFuncSetAttribute((const void*)mega_kernel, hipFuncAttributeMaxDynamicSharedMemorySize, CH_LDS);
    mega_kernel<<<256, 512, CH_LDS, stream>>>(a);
}
```

```cpp
#include <hip/hip_runtime.h>
#include <stdint.h>
#define LAS __attribute__((address_space(3)))
#define GAS __attribute__((address_space(1)))
typedef unsigned short bf16_t;
typedef short bf16x8 __attribute__((ext_vector_type(8)));
typedef short bf16x4 __attribute__((ext_vector_type(4)));
typedef float f32x4 __attribute__((ext_vector_type(4)));
typedef float f32x2 __attribute__((ext_vector_type(2)));
typedef unsigned u32x4 __attribute__((ext_vector_type(4)));
typedef unsigned u32x2 __attribute__((ext_vector_type(2)));
typedef __bf16 bf16v2 __attribute__((ext_vector_type(2)));

__device__ __forceinline__ unsigned cvt_pk_bf16(float lo, float hi) { f32x2 v = {lo, hi}; bf16v2 r = __builtin_convertvector(v, bf16v2); return __builtin_bit_cast(unsigned, r); }
__device__ __forceinline__ float rbf(float x) { return __uint_as_float(cvt_pk_bf16(x, x) << 16); }
__device__ __forceinline__ float bflo(unsigned w) { return __uint_as_float(w << 16); }
__device__ __forceinline__ float bfhi(unsigned w) { return __uint_as_float(w & 0xFFFF0000u); }

constexpr int S_ = 2048, DM = 1024, NH = 32, DH = 128, NSEG = 64, FF = 3584;
constexpr unsigned SENT = 0xFFFFFFFFu;

struct ChainArgs {
    const bf16_t* mem0; const bf16_t* Wq; const bf16_t* Wk; const bf16_t* Wv; const bf16_t* Wo;
    const bf16_t* qkv;
    unsigned* membuf;
    unsigned* Ubuf;
    unsigned* Xbuf;
    bf16_t* kvmem;
    unsigned* tmo;
    unsigned* ctr;
    unsigned* slots;
    unsigned* tail;
    int lag; int nstep;
    unsigned qstride; unsigned pad0;
};
#define STAMP(i) do {} while (0)
#define LBAR() do { asm volatile("s_waitcnt lgkmcnt(0)" ::: "memory"); __builtin_amdgcn_s_barrier(); asm volatile("" ::: "memory"); } while (0)
#define CH_CAP 100000u
#define WK_CAP 20000u
#define CH_WL 0
#define CH_OV (128*1024)
#define CH_LDS (160*1024)

__device__ __forceinline__ u32x4 ld16_sc1(__amdgpu_buffer_rsrc_t r, unsigned off) { asm volatile("" ::: "memory"); u32x4 v = __builtin_amdgcn_raw_buffer_load_b128(r, off, 0, 16); asm volatile("" ::: "memory"); return v; }
__device__ __forceinline__ void st16_sc1(u32x4 v, __amdgpu_buffer_rsrc_t r, unsigned off) { __builtin_amdgcn_raw_buffer_store_b128(v, r, off, 0, 16); }
__device__ __forceinline__ void st8b_sc1(u32x2 v, __amdgpu_buffer_rsrc_t r, unsigned off) { __builtin_amdgcn_raw_buffer_store_b64(v, r, off, 0, 16); }
__device__ __forceinline__ void st8_sc1(void* p, u32x2 v) { __hip_atomic_store((GAS unsigned long long*)p, ((unsigned long long)v.y << 32) | v.x, __ATOMIC_RELAXED, __HIP_MEMORY_SCOPE_AGENT); }
__device__ __forceinline__ void st4_sc1(unsigned* p, unsigned v) { __hip_atomic_store((GAS unsigned*)p, v, __ATOMIC_RELAXED, __HIP_MEMORY_SCOPE_AGENT); }

enum { PH_QKV = 0, PH_ATT = 1, PH_OUT = 2, PH_REDA = 3, PH_FFN1 = 4, PH_FFN2 = 5, PH_REDB = 6, PH_ARDY = 7 };
__device__ __forceinline__ constexpr int ph_count(int ph) { return ph == PH_QKV ? 48 : ph == PH_ATT ? 16 : ph == PH_OUT ? 32 : ph == PH_REDA ? 16 : ph == PH_FFN1 ? 14 : ph == PH_FFN2 ? 28 : 16; }
__device__ __forceinline__ unsigned wave_add_ret(unsigned* p0, unsigned v, int lane) {
    const unsigned long long pa = (unsigned long long)p0;
    unsigned* p = (unsigned*)(((unsigned long long)(unsigned)__builtin_amdgcn_readfirstlane((unsigned)(pa >> 32)) << 32) | (unsigned)__builtin_amdgcn_readfirstlane((unsigned)pa));
    const unsigned old = __hip_atomic_fetch_add((GAS unsigned*)p, lane == 0 ? v : 0u, __ATOMIC_RELAXED, __HIP_MEMORY_SCOPE_AGENT); return __builtin_amdgcn_readfirstlane(old); }
__device__ __forceinline__ void push_group(unsigned* slots, unsigned* tail, int ph, int l, int b, int n, int lane) {
    const unsigned base = wave_add_ret(tail, (unsigned)n, lane);
    if (lane < n) st4_sc1(slots + (base + lane) * 16, 1u + ((unsigned)ph | ((unsigned)l << 3) | ((unsigned)b << 4) | ((unsigned)lane << 8)));
}
typedef short v4s __attribute__((ext_vector_type(4)));
__device__ __forceinline__ bf16x8 tr_frag_at(LAS unsigned char* a0) {
    v4s lo = __builtin_amdgcn_ds_read_tr16_b64_v4i16((LAS v4s*)a0);
    v4s hi = __builtin_amdgcn_ds_read_tr16_b64_v4i16((LAS v4s*)(a0 + 4 * 256));
    bf16x8 r; r[0] = lo[0]; r[1] = lo[1]; r[2] = lo[2]; r[3] = lo[3]; r[4] = hi[0]; r[5] = hi[1]; r[6] = hi[2]; r[7] = hi[3];
    return r;
}
template <int CTRL> __device__ __forceinline__ float dpp_f(float x) { return __int_as_float(__builtin_amdgcn_update_dpp(0, __float_as_int(x), CTRL, 0xF, 0xF, false)); }
__device__ __forceinline__ float row16_max(float x) { x = fmaxf(x, dpp_f<0x128>(x)); x = fmaxf(x, dpp_f<0x124>(x)); x = fmaxf(x, dpp_f<0x122>(x)); x = fmaxf(x, dpp_f<0x121>(x)); return x; }
__device__ __forceinline__ float row16_sum(float x) { x += dpp_f<0x128>(x); x += dpp_f<0x124>(x); x += dpp_f<0x122>(x); x += dpp_f<0x121>(x); return x; }

__device__ __forceinline__ void chain_role(const ChainArgs& a, LAS unsigned char* lds, int c) {
    const int tid = threadIdx.x, w = __builtin_amdgcn_readfirstlane(tid >> 6);
    const int h = c >> 1, hf = c & 1, kh = w >> 2;
    bf16x8 wqk[32], wv[16];
    {
        const int lane = tid & 63;
        if (w == 0) {
            const int tok = lane & 15, g4 = lane >> 4;
            u32x2 o = *(const u32x2*)(a.mem0 + (tok & 7) * 1024 + c * 16 + g4 * 4);
            if (o.x == SENT) o.x = 0x7FC07FC0u; if (o.y == SENT) o.y = 0x7FC07FC0u;
            st8_sc1((void*)(a.membuf + (c >> 1) * 256 + (2 * (c & 1) + (g4 >> 1)) * 64 + tok * 4 + (g4 & 1) * 2), o);
        }
        const int g = lane >> 4, q = (lane & 15) >> 2, p = lane & 3;
        const int row = tid >> 4, ch = tid & 15;
#pragma unroll
        for (int grp = 0; grp < 2; ++grp) {
            const bf16_t* src = (ch < 8 ? a.Wq : a.Wk) + (size_t)(grp * 512 + row) * 4096 + h * 128 + hf * 64 + (ch & 7) * 8;
#pragma unroll
            for (int i = 0; i < 16; ++i)
                __builtin_amdgcn_global_load_lds((const unsigned*)(src + (size_t)i * 32 * 4096), (LAS unsigned*)(lds + i * 8192 + w * 1024), 16, 0, 0);
            asm volatile("s_waitcnt vmcnt(0)" ::: "memory");
            __syncthreads();
            LAS unsigned char* a0 = lds + (8 * g + q) * 256 + ((p < 2 ? 8 * w + 4 * p : 64 + 8 * w + 4 * (p - 2))) * 2;
            if (grp == kh) {
#pragma unroll
                for (int i = 0; i < 16; ++i) wqk[i] = tr_frag_at(a0 + i * 8192);
            } else {
#pragma unroll
                for (int i = 0; i < 16; ++i) wqk[16 + i] = tr_frag_at(a0 + i * 8192);
            }
            asm volatile("s_waitcnt lgkmcnt(0)" ::: "memory");
            __syncthreads();
        }
        {
            const bf16_t* src = a.Wv + (size_t)((ch >> 3) * 512 + row) * 4096 + h * 128 + hf * 64 + (ch & 7) * 8;
#pragma unroll
            for (int i = 0; i < 16; ++i)
                __builtin_amdgcn_global_load_lds((const unsigned*)(src + (size_t)i * 32 * 4096), (LAS unsigned*)(lds + i * 8192 + w * 1024), 16, 0, 0);
            asm volatile("s_waitcnt vmcnt(0)" ::: "memory");
            __syncthreads();
            LAS unsigned char* a0 = lds + (8 * g + q) * 256 + (64 * kh + 16 * (w & 3) + 4 * p) * 2;
#pragma unroll
            for (int i = 0; i < 16; ++i) wv[i] = tr_frag_at(a0 + i * 8192);
            asm volatile("s_waitcnt lgkmcnt(0)" ::: "memory");
            __syncthreads();
        }
#pragma unroll 1
        for (int cg = 0; cg < 4; ++cg) {
            const bf16_t* src = a.Wo + (size_t)(h * 128 + row) * 1024 + hf * 512 + cg * 128 + ch * 8;
#pragma unroll
            for (int ks = 0; ks < 4; ++ks)
                __builtin_amdgcn_global_load_lds((const unsigned*)(src + (size_t)ks * 32 * 1024), (LAS unsigned*)(lds + CH_OV + ks * 8192 + w * 1024), 16, 0, 0);
            asm volatile("s_waitcnt vmcnt(0)" ::: "memory");
            __syncthreads();
            LAS unsigned char* a0 = lds + CH_OV + (8 * g + q) * 256 + (16 * w + 4 * p) * 2;
#pragma unroll
            for (int ks = 0; ks < 4; ++ks) { bf16x8 f = tr_frag_at(a0 + ks * 8192);
                *(LAS bf16x8*)(lds + CH_WL + w * 16384 + (4 * cg + ks) * 1024 + lane * 16) = f; }
            asm volatile("s_waitcnt lgkmcnt(0)" ::: "memory");
            __syncthreads();
        }
    }
    const int T = a.nstep, L = a.lag;
    __amdgpu_buffer_rsrc_t rs_mem = __builtin_amdgcn_make_buffer_rsrc((void*)a.membuf, 0, (T + 1) * 32768, 0x00020000);
    __amdgpu_buffer_rsrc_t rs_U = __builtin_amdgcn_make_buffer_rsrc((void*)a.Ubuf, 0, 64 * 32768, 0x00020000);
    __amdgpu_buffer_rsrc_t rs_X = __builtin_amdgcn_make_buffer_rsrc((void*)a.Xbuf, 0, 64 * 8192, 0x00020000);
    __amdgpu_buffer_rsrc_t rs_q = __builtin_amdgcn_make_buffer_rsrc((void*)a.qkv, 0, a.qstride + 2048u * 24576u, 0x00020000);
    __amdgpu_buffer_rsrc_t rs_kv = __builtin_amdgcn_make_buffer_rsrc((void*)a.kvmem, 0, 4 * 64 * 32 * 2048, 0x00020000);
    LAS unsigned char* ov = lds + CH_OV;
    bool dead = false;
    int pend = -1; unsigned pbase = 0;
#pragma unroll 1
    for (int t = 0; t <= T; ++t) {
        int lane = tid & 63; asm volatile("" : "+v"(lane));
        const int tok = lane & 15, g4 = lane >> 4, slot = tok >> 3;
        const int s0 = t < 64 ? t : 63, s1 = t - L < 0 ? 0 : (t - L > 63 ? 63 : t - L);
        const bool act0 = t < 64, act1 = t >= L && t - L < 64;
        const int sseg = slot ? s1 : s0; const bool act = slot ? act1 : act0;
        const unsigned tag = t & 1;
        STAMP(0);
        unsigned rsv = 0;
        const bool ann = (c == 0 && act0 && (s0 & 3) == 3) || (c == 2 && act1 && (s1 & 3) == 3);
        if (w == 1 && ann) rsv = __hip_atomic_fetch_add((GAS unsigned*)a.tail, lane == 0 ? 16u : 0u, __ATOMIC_RELAXED, __HIP_MEMORY_SCOPE_AGENT);
        if ((act0 && (s0 & 3) == 0) || (act1 && (s1 & 3) == 0)) {
            unsigned spins = 0;
            for (;;) {
                unsigned v0 = 48u, v1 = 48u;
                if (act0 && (s0 & 3) == 0) v0 = __hip_atomic_load((GAS unsigned*)(a.ctr + ((0 * 8 + 0) * 16 + (s0 >> 2)) * 64), __ATOMIC_RELAXED, __HIP_MEMORY_SCOPE_AGENT);
                if (act1 && (s1 & 3) == 0) v1 = __hip_atomic_load((GAS unsigned*)(a.ctr + ((1 * 8 + 0) * 16 + (s1 >> 2)) * 64), __ATOMIC_RELAXED, __HIP_MEMORY_SCOPE_AGENT);
                if (__builtin_amdgcn_readfirstlane(v0) >= 48u && __builtin_amdgcn_readfirstlane(v1) >= 48u) break;
                if (dead || ++spins > CH_CAP) { dead = true; if (lane == 0) st4_sc1(a.tmo, 5u); break; }
            }
            asm volatile("" ::: "memory");
        }
        u32x2 kx[2];
        const unsigned qo0 = act0 ? (unsigned)s0 * 786432u + h * 768 : 0x80000000u, qo1 = act1 ? (unsigned)s1 * 786432u + a.qstride + h * 768 : 0x80000000u;
        {   const unsigned kp = ((g4 >> 1) ? qo1 : qo0) + tok * 24576 + (128 + hf * 64 + 8 * w + 4 * (g4 & 1)) * 2;
            kx[0] = __builtin_amdgcn_raw_buffer_load_b64(rs_q, kp, 0, 16); kx[1] = __builtin_amdgcn_raw_buffer_load_b64(rs_q, kp + 16 * 24576, 0, 16); }
        const unsigned vp = (kh ? qo1 : qo0) + ((w & 3) * 8 + (lane >> 3)) * 24576 + 512 + (lane & 7) * 32;
        const u32x4 vx0 = __builtin_amdgcn_raw_buffer_load_b128(rs_q, vp, 0, 16), vx1 = __builtin_amdgcn_raw_buffer_load_b128(rs_q, vp + 16, 0, 16);
        {
            u32x4 v[4];
            {
                unsigned spins = 0;
                for (;;) {
                    bool ok = true;
#pragma unroll
                    for (int i = 0; i < 4; ++i) v[i] = ld16_sc1(rs_mem, t * 32768 + w * 4096 + i * 1024 + lane * 16);
#pragma unroll
                    for (int i = 0; i < 4; ++i) ok = ok && v[i].x != SENT && v[i].y != SENT && v[i].z != SENT && v[i].w != SENT;
                    if (__all(ok)) break;
                    if (dead || ++spins > CH_CAP) { dead = true; if (lane == 0) st4_sc1(a.tmo, 1u); break; }
                }
            }
#pragma unroll
            for (int i = 0; i < 4; ++i) *(LAS u32x4*)(ov + w * 4096 + i * 1024 + lane * 16) = v[i];
        }
        STAMP(1);
        LBAR();
        STAMP(2);
        if (w == 1 && pend >= 0) { if (lane < 16) st4_sc1(a.slots + (pbase + lane) * 16, 1u + ((unsigned)PH_ATT | ((unsigned)(c >> 1) << 3) | ((unsigned)pend << 4) | ((unsigned)lane << 8))); }
        if (t == T) break;
        pend = -1;
        if (w == 1 && ann) { pend = (c == 0 ? s0 : s1) >> 2; pbase = __builtin_amdgcn_readfirstlane(rsv); }
        f32x4 aqk = {0.f, 0.f, 0.f, 0.f}, av = {0.f, 0.f, 0.f, 0.f};
        {
            LAS unsigned char* mf = ov + lane * 16;
            const int kofs = kh * 16384;
            bf16x8 bb[3];
            bb[0] = *(LAS bf16x8*)(mf + kofs); bb[1] = *(LAS bf16x8*)(mf + kofs + 1024);
#pragma unroll
            for (int i = 0; i < 32; ++i) {
                if (i + 2 < 32) bb[(i + 2) % 3] = *(LAS bf16x8*)(mf + (((i + 2) * 1024) & 16383) + (i + 2 < 16 ? kofs : (16384 - kofs)));
                __builtin_amdgcn_sched_barrier(0);
                aqk = __builtin_amdgcn_mfma_f32_16x16x32_bf16(wqk[i], bb[i % 3], aqk, 0, 0, 0);
                if (i < 16) av = __builtin_amdgcn_mfma_f32_16x16x32_bf16(wv[i], bb[i % 3], av, 0, 0, 0);
                __builtin_amdgcn_sched_barrier(0);
            }
        }
        STAMP(3);
        u32x2 x2; x2.x = cvt_pk_bf16(aqk[0], aqk[1]); x2.y = cvt_pk_bf16(aqk[2], aqk[3]);
        const unsigned kvo = (unsigned)(((slot * 2) * 64 + sseg) * 32 + h) * 2048 + (tok & 7) * 256 + hf * 128;
        if (act && g4 >= 2) st8b_sc1(x2, rs_kv, kvo + 16 * w + 8 * (g4 - 2));
        f32x4 sm, sx0, sx1;
        {
            auto r0 = __builtin_amdgcn_permlane32_swap(x2.x, x2.x, false, false);
            auto r1 = __builtin_amdgcn_permlane32_swap(x2.y, x2.y, false, false);
            u32x2 qa, kb; qa.x = r0[0]; qa.y = r1[0]; kb.x = r0[1]; kb.y = r1[1];
            if ((g4 >> 1) != slot) { qa.x = 0u; qa.y = 0u; }
            const f32x4 z = {0.f, 0.f, 0.f, 0.f};
            sm = __builtin_amdgcn_mfma_f32_16x16x16bf16_1k(__builtin_bit_cast(bf16x4, kb), __builtin_bit_cast(bf16x4, qa), z, 0, 0, 0);
            sx0 = __builtin_amdgcn_mfma_f32_16x16x16bf16_1k(__builtin_bit_cast(bf16x4, kx[0]), __builtin_bit_cast(bf16x4, qa), z, 0, 0, 0);
            sx1 = __builtin_amdgcn_mfma_f32_16x16x16bf16_1k(__builtin_bit_cast(bf16x4, kx[1]), __builtin_bit_cast(bf16x4, qa), z, 0, 0, 0);
        }
        STAMP(4);
        LBAR();
        {
            LAS unsigned char* sp = ov + w * 2560 + tok * 160;
            if ((g4 >> 1) == slot) *(LAS f32x4*)(sp + (g4 & 1) * 16) = sm;
            *(LAS f32x4*)(sp + 32 + g4 * 16) = sx0; *(LAS f32x4*)(sp + 96 + g4 * 16) = sx1;
        }
        if (w < 4) *(LAS f32x4*)(ov + 20480 + w * 1024 + lane * 16) = av;
        LBAR();
        STAMP(5);
        LAS unsigned char* vxd = ov + kh * 10880 + (8 + (w & 3) * 8 + (lane >> 3)) * 272 + (lane & 7) * 32;
        if (w >= 4) {
            av += *(LAS f32x4*)(ov + 20480 + (w - 4) * 1024 + lane * 16);
            u32x2 vb; vb.x = cvt_pk_bf16(av[0], av[1]); vb.y = cvt_pk_bf16(av[2], av[3]);
            { u32x4 o; o.x = (vb.x << 16) | tag; o.y = (vb.x & 0xFFFF0000u) | tag; o.z = (vb.y << 16) | tag; o.w = (vb.y & 0xFFFF0000u) | tag;
                st16_sc1(o, rs_X, (unsigned)(c * 8192 + 4096 + (w - 4) * 1024 + lane * 16)); }
            if (act) st8b_sc1(vb, rs_kv, kvo + 64 * 32 * 2048 + 32 * (w - 4) + 8 * g4);
            STAMP(6);
            LBAR();
            *(LAS u32x2*)(ov + slot * 10880 + (tok & 7) * 272 + (hf * 64 + 16 * (w - 4) + 4 * g4) * 2) = vb;
            *(LAS u32x4*)vxd = vx0; *(LAS u32x4*)(vxd + 16) = vx1;
            STAMP(7);
            u32x4 pv; unsigned spins = 0;
            for (;;) {
                pv = ld16_sc1(rs_X, (unsigned)((c ^ 1) * 8192 + 4096 + (w - 4) * 1024 + lane * 16));
                if (__all(((pv.x ^ tag) | (pv.y ^ tag) | (pv.z ^ tag) | (pv.w ^ tag)) & 1u ? false : true)) break;
                if (dead || ++spins > CH_CAP) { dead = true; if (lane == 0) st4_sc1(a.tmo, 3u); break; }
            }
            u32x2 o; o.x = (pv.x >> 16) | (pv.y & 0xFFFF0000u); o.y = (pv.z >> 16) | (pv.w & 0xFFFF0000u);
            *(LAS u32x2*)(ov + slot * 10880 + (tok & 7) * 272 + ((hf ^ 1) * 64 + 16 * (w - 4) + 4 * g4) * 2) = o;
        } else {
            const int qt = 4 * w + g4, gr = tok < 10 ? tok : 9;
            f32x4 own = (f32x4){0.f, 0.f, 0.f, 0.f};
#pragma unroll
            for (int ww = 0; ww < 8; ++ww) own += *(LAS f32x4*)(ov + ww * 2560 + qt * 160 + gr * 16);
            u32x4 o; o.x = (__float_as_uint(own[0]) & ~1u) | tag; o.y = (__float_as_uint(own[1]) & ~1u) | tag; o.z = (__float_as_uint(own[2]) & ~1u) | tag; o.w = (__float_as_uint(own[3]) & ~1u) | tag;
            if (tok < 10) st16_sc1(o, rs_X, (unsigned)(c * 8192 + qt * 160 + tok * 16));
            STAMP(6);
            LBAR();
            *(LAS u32x4*)vxd = vx0; *(LAS u32x4*)(vxd + 16) = vx1;
            STAMP(7);
            u32x4 ps; unsigned spins = 0;
            for (;;) {
                ps = ld16_sc1(rs_X, (unsigned)((c ^ 1) * 8192 + qt * 160 + gr * 16));
                if (__all(((ps.x ^ tag) | (ps.y ^ tag) | (ps.z ^ tag) | (ps.w ^ tag)) & 1u ? false : true)) break;
                if (dead || ++spins > CH_CAP) { dead = true; if (lane == 0) st4_sc1(a.tmo, 4u); break; }
            }
            float sv[4]; float m = -3.0e38f;
#pragma unroll
            for (int j = 0; j < 4; ++j) { const float s = __uint_as_float(o[j] & ~1u) + __uint_as_float(ps[j] & ~1u); sv[j] = rbf(rbf(s) * 0.08838834764831845f); m = fmaxf(m, sv[j]); }
            if (tok >= 10) m = -3.0e38f;
            m = row16_max(m);
            float e[4], d = 0.f;
#pragma unroll
            for (int j = 0; j < 4; ++j) { e[j] = tok < 10 ? __expf(sv[j] - m) : 0.f; d += e[j]; }
            d = row16_sum(d);
            const float inv = 1.0f / d;
            u32x2 pw; pw.x = cvt_pk_bf16(e[0] * inv, e[1] * inv); pw.y = cvt_pk_bf16(e[2] * inv, e[3] * inv);
            if (tok < 12) *(LAS u32x2*)(ov + 21760 + qt * 96 + tok * 8) = pw;
        }
        STAMP(8);
        LBAR();
        STAMP(9);
        {
            const u32x4 p0 = *(LAS u32x4*)(ov + 21760 + tok * 96 + g4 * 16);
            const u32x2 p1 = *(LAS u32x2*)(ov + 21760 + tok * 96 + 64 + g4 * 8);
            f32x4 oa = {0.f, 0.f, 0.f, 0.f};
            const int q4 = (lane & 15) >> 2, p4 = lane & 3;
#pragma unroll
            for (int cc = 0; cc < 2; ++cc) {
                LAS unsigned char* a0 = ov + cc * 10880 + (8 * g4 + q4) * 272 + (16 * w + 4 * p4) * 2;
                v4s lo = __builtin_amdgcn_ds_read_tr16_b64_v4i16((LAS v4s*)a0);
                v4s hi = __builtin_amdgcn_ds_read_tr16_b64_v4i16((LAS v4s*)(a0 + 4 * 272));
                int r2 = 32 + 4 * g4 + q4; if (r2 > 39) r2 = 39;
                v4s l2 = __builtin_amdgcn_ds_read_tr16_b64_v4i16((LAS v4s*)(ov + cc * 10880 + r2 * 272 + (16 * w + 4 * p4) * 2));
                bf16x8 vf; vf[0] = lo[0]; vf[1] = lo[1]; vf[2] = lo[2]; vf[3] = lo[3]; vf[4] = hi[0]; vf[5] = hi[1]; vf[6] = hi[2]; vf[7] = hi[3];
                u32x4 pa = p0; u32x2 pb = p1; if (slot != cc) { pa = (u32x4){0u, 0u, 0u, 0u}; pb = (u32x2){0u, 0u}; }
                oa = __builtin_amdgcn_mfma_f32_16x16x32_bf16(vf, __builtin_bit_cast(bf16x8, pa), oa, 0, 0, 0);
                oa = __builtin_amdgcn_mfma_f32_16x16x16bf16_1k(l2, __builtin_bit_cast(bf16x4, pb), oa, 0, 0, 0);
            }
            u32x2 ob; ob.x = cvt_pk_bf16(oa[0], oa[1]); ob.y = cvt_pk_bf16(oa[2], oa[3]);
            *(LAS u32x2*)(ov + 24576 + (w >> 1) * 1024 + (2 * (w & 1) + (g4 >> 1)) * 256 + tok * 16 + (g4 & 1) * 8) = ob;
        }
        LAS unsigned char* wl = lds + CH_WL + w * 16384 + lane * 16;
        __builtin_amdgcn_sched_barrier(0);
        bf16x8 wa[4];
#pragma unroll
        for (int i = 0; i < 4; ++i) wa[i] = *(LAS bf16x8*)(wl + i * 1024);
        __builtin_amdgcn_sched_barrier(0);
        LBAR();
        STAMP(10);
        {
            bf16x8 of[4];
#pragma unroll
            for (int ks = 0; ks < 4; ++ks) of[ks] = *(LAS bf16x8*)(ov + 24576 + ks * 1024 + lane * 16);
            const unsigned uo = (unsigned)(((hf * 32 + w) * 32 + h) * 1024 + tok * 64 + g4 * 16);
#pragma unroll
            for (int cg = 0; cg < 4; ++cg) {
                f32x4 u = {0.f, 0.f, 0.f, 0.f};
#pragma unroll
                for (int ks = 0; ks < 4; ++ks) { u = __builtin_amdgcn_mfma_f32_16x16x32_bf16(wa[ks], of[ks], u, 0, 0, 0);
                    __builtin_amdgcn_sched_barrier(0);
                    if (cg < 3) wa[ks] = *(LAS bf16x8*)(wl + ((cg + 1) * 4 + ks) * 1024);
                    __builtin_amdgcn_sched_barrier(0); }
                u32x4 o; o.x = (__float_as_uint(u[0]) & ~1u) | tag; o.y = (__float_as_uint(u[1]) & ~1u) | tag; o.z = (__float_as_uint(u[2]) & ~1u) | tag; o.w = (__float_as_uint(u[3]) & ~1u) | tag;
                st16_sc1(o, rs_U, uo + cg * (8 * 32 * 1024));
                __builtin_amdgcn_sched_barrier(0);
            }
        }
        STAMP(11);
        {
            u32x4 uu[4]; unsigned spins = 0;
            const unsigned uoff = (unsigned)(c * 32768 + (4 * w) * 1024 + tok * 64 + g4 * 16);
            for (;;) {
#pragma unroll
                for (int i = 0; i < 4; ++i) uu[i] = ld16_sc1(rs_U, uoff + i * 1024);
                const unsigned m = tag ? 0xFFFFFFFFu : 0u; unsigned x = 0;
#pragma unroll
                for (int i = 0; i < 4; ++i) x |= (uu[i].x ^ m) | (uu[i].y ^ m) | (uu[i].z ^ m) | (uu[i].w ^ m);
                if (__all((x & 1u) == 0u)) break;
                if (dead || ++spins > CH_CAP) { dead = true; if (lane == 0) st4_sc1(a.tmo, 2u); break; }
            }
            STAMP(12);
            f32x4 r0;
#pragma unroll
            for (int j = 0; j < 4; ++j) r0[j] = (__uint_as_float(uu[0][j] & ~1u) + __uint_as_float(uu[1][j] & ~1u)) + (__uint_as_float(uu[2][j] & ~1u) + __uint_as_float(uu[3][j] & ~1u));
            LAS unsigned char* red = ov;
            *(LAS f32x4*)(red + w * 1024 + lane * 16) = r0;
            asm volatile("s_waitcnt vmcnt(0)" ::: "memory");
            LBAR();
            STAMP(13);
            if (w == 0) {
                f32x4 tt = {0.f, 0.f, 0.f, 0.f};
#pragma unroll
                for (int ww = 0; ww < 8; ++ww) tt += *(LAS f32x4*)(red + ww * 1024 + lane * 16);
                u32x2 o; o.x = cvt_pk_bf16(tt[0], tt[1]); o.y = cvt_pk_bf16(tt[2], tt[3]);
                if (t + 1 == L && tok >= 8) o = *(const u32x2*)(a.mem0 + (tok & 7) * 1024 + c * 16 + g4 * 4);
                if (o.x == SENT) o.x = 0x7FC07FC0u; if (o.y == SENT) o.y = 0x7FC07FC0u;
                st8b_sc1(o, rs_mem, (unsigned)((t + 1) * 32768 + (c >> 1) * 1024 + (2 * (c & 1) + (g4 >> 1)) * 256 + tok * 16 + (g4 & 1) * 8));
            }
        }
    }
}

__host__ __device__ __forceinline__ int lds_byte(int r, int c) { const int st = (r >> 4) * 2 + (c >> 5), rr = r & 15, cc = c & 31, ob = rr * 64 + cc * 2; return st * 1024 + (ob ^ (((ob >> 9) & 1) << 5)); }
__host__ __device__ __forceinline__ void stage_rc(int b, int& R, int& C) { const int st = b / 1024, sb = b % 1024, swz = sb ^ (((sb >> 9) & 1) << 5); R = (st >> 1) * 16 + swz / 64; C = (st & 1) * 32 + (swz % 64) / 2; }
__host__ __device__ __forceinline__ int perm32(int rho) { const int n = rho >> 4, i = rho & 15; return 8 * (i >> 2) + 4 * n + (i & 3); }

template <int N_> __device__ __forceinline__ void wait_v() {
    if constexpr (N_ == 0) asm volatile("s_waitcnt vmcnt(0)" ::: "memory");
    else if constexpr (N_ == 1) asm volatile("s_waitcnt vmcnt(1)" ::: "memory");
    else if constexpr (N_ == 2) asm volatile("s_waitcnt vmcnt(2)" ::: "memory");
    else if constexpr (N_ == 3) asm volatile("s_waitcnt vmcnt(3)" ::: "memory");
    else if constexpr (N_ == 4) asm volatile("s_waitcnt vmcnt(4)" ::: "memory");
    else if constexpr (N_ == 5) asm volatile("s_waitcnt vmcnt(5)" ::: "memory");
    else if constexpr (N_ == 6) asm volatile("s_waitcnt vmcnt(6)" ::: "memory");
}

template <int MT, bool PERM>
__device__ __forceinline__ void gemm_tile(LAS unsigned char* lds, const bf16_t* A, int lda, const bf16_t* Bt, int ldb, int nt, f32x4 (&acc)[2][2][MT][2]) {
    constexpr int NA = MT / 2;
    constexpr int HA = MT * 32;
    constexpr int HTA = HA * 128;
    constexpr int HTB = 16384;
    const int tid = threadIdx.x, wid = __builtin_amdgcn_readfirstlane(tid >> 6), lane = tid & 63, wr = wid >> 2, wc = wid & 3, fr = lane & 15, fq = lane >> 4;
    unsigned voffA[NA], voffB[2];
#pragma unroll
    for (int i = 0; i < NA; ++i) { int R, C; stage_rc(tid * 16 + i * 8192, R, C); voffA[i] = (unsigned)(R * lda + C) * 2u; }
#pragma unroll
    for (int i = 0; i < 2; ++i) { int R, C; stage_rc(tid * 16 + i * 8192, R, C); const int Rb = PERM ? ((R & ~31) + perm32(R & 31)) : R; voffB[i] = (unsigned)(Rb * ldb + C) * 2u; }
    const size_t kstep = 128;
    const size_t hstepA = (size_t)HA * lda * 2, hstepB = (size_t)128 * ldb * 2;
    const unsigned ldsw = (unsigned)wid * 1024u;
    const int aoff = lds_byte(wr * MT * 16 + fr, fq * 8), boff = lds_byte(wc * 32 + fr, fq * 8);
    const char* cA = (const char*)A; const char* cB = (const char*)Bt;
#define SA_(b, h) (((b) * 2 + (h)) * HTA)
#define SB_(b, h) (4 * HTA + ((b) * 2 + (h)) * HTB)
#define STAGE_A(b, h, kt) do { _Pragma("unroll") for (int _i = 0; _i < NA; ++_i) \
        __builtin_amdgcn_global_load_lds((const unsigned*)(cA + (h) * hstepA + (size_t)(kt) * kstep + voffA[_i]), (LAS unsigned*)(lds + SA_(b, h) + ldsw + _i * 8192), 16, 0, 0); } while (0)
#define STAGE_B(b, h, kt) do { _Pragma("unroll") for (int _i = 0; _i < 2; ++_i) \
        __builtin_amdgcn_global_load_lds((const unsigned*)(cB + (h) * hstepB + (size_t)(kt) * kstep + voffB[_i]), (LAS unsigned*)(lds + SB_(b, h) + ldsw + _i * 8192), 16, 0, 0); } while (0)
#define LDA_(dst, b, h) do { _Pragma("unroll") for (int m = 0; m < MT; ++m) _Pragma("unroll") for (int k = 0; k < 2; ++k) dst[m][k] = *(const LAS bf16x8*)(lds + SA_(b, h) + aoff + m * 2048 + k * 1024); } while (0)
#define LDB_(dst, b, h) do { _Pragma("unroll") for (int n = 0; n < 2; ++n) _Pragma("unroll") for (int k = 0; k < 2; ++k) dst[n][k] = *(const LAS bf16x8*)(lds + SB_(b, h) + boff + n * 2048 + k * 1024); } while (0)
#define MMA_(ai, bj, At, Bq) do { __builtin_amdgcn_s_setprio(1); _Pragma("unroll") for (int m = 0; m < MT; ++m) _Pragma("unroll") for (int n = 0; n < 2; ++n) _Pragma("unroll") for (int k = 0; k < 2; ++k) \
        acc[ai][bj][m][n] = __builtin_amdgcn_mfma_f32_16x16x32_bf16(Bq[n][k], At[m][k], acc[ai][bj][m][n], 0, 0, 0); __builtin_amdgcn_s_setprio(0); } while (0)
#define WAIT_L(n) asm volatile("s_waitcnt lgkmcnt(" #n ")" ::: "memory")
#define BAR __builtin_amdgcn_s_barrier()
#define SCHED __builtin_amdgcn_sched_barrier(0)
#pragma unroll
    for (int a = 0; a < 2; ++a)
#pragma unroll
        for (int b = 0; b < 2; ++b)
#pragma unroll
            for (int m = 0; m < MT; ++m)
#pragma unroll
                for (int n = 0; n < 2; ++n) acc[a][b][m][n] = (f32x4){0.f, 0.f, 0.f, 0.f};
    bf16x8 At[MT][2], B0[2][2], B1[2][2];
    STAGE_B(0, 0, 0); STAGE_A(0, 0, 0); STAGE_B(0, 1, 0); STAGE_A(0, 1, 0);
    if (wr == 1) BAR;
    wait_v<2 + NA>(); BAR;
    STAGE_B(1, 0, 1); STAGE_A(1, 0, 1); STAGE_B(1, 1, 1);
    wait_v<4 + NA>(); BAR;
    for (int t = 0; t < nt - 2; t += 2) {
        LDB_(B0, 0, 0); SCHED; LDA_(At, 0, 0); STAGE_A(1, 1, t + 1);
        if constexpr (MT == 4) WAIT_L(8); else WAIT_L(4);
        BAR; WAIT_L(0); MMA_(0, 0, At, B0); BAR; SCHED;
        LDB_(B1, 0, 1); STAGE_B(0, 0, t + 2);
        BAR; WAIT_L(0); MMA_(0, 1, At, B1); BAR;
        LDA_(At, 0, 1); STAGE_A(0, 0, t + 2);
        BAR; WAIT_L(0); MMA_(1, 0, At, B0); BAR; SCHED;
        STAGE_B(0, 1, t + 2);
        wait_v<4 + NA>(); BAR; MMA_(1, 1, At, B1); BAR;
        LDB_(B0, 1, 0); SCHED; LDA_(At, 1, 0); STAGE_A(0, 1, t + 2);
        if constexpr (MT == 4) WAIT_L(8); else WAIT_L(4);
        BAR; WAIT_L(0); MMA_(0, 0, At, B0); BAR; SCHED;
        LDB_(B1, 1, 1); STAGE_B(1, 0, t + 3);
        BAR; WAIT_L(0); MMA_(0, 1, At, B1); BAR;
        LDA_(At, 1, 1); STAGE_A(1, 0, t + 3);
        BAR; WAIT_L(0); MMA_(1, 0, At, B0); BAR; SCHED;
        STAGE_B(1, 1, t + 3);
        wait_v<4 + NA>(); BAR; MMA_(1, 1, At, B1); BAR;
    }
    { LDB_(B0, 0, 0); wait_v<0>(); LDA_(At, 0, 0); STAGE_A(1, 1, nt - 1);
      BAR; WAIT_L(0); MMA_(0, 0, At, B0); BAR;
      LDB_(B1, 0, 1); BAR; WAIT_L(0); MMA_(0, 1, At, B1); BAR;
      LDA_(At, 0, 1); BAR; WAIT_L(0); MMA_(1, 0, At, B0); MMA_(1, 1, At, B1); BAR; }
    { LDB_(B0, 1, 0); LDA_(At, 1, 0); BAR; WAIT_L(0); MMA_(0, 0, At, B0); BAR;
      LDB_(B1, 1, 1); wait_v<0>(); BAR; WAIT_L(0); MMA_(0, 1, At, B1); BAR;
      LDA_(At, 1, 1); BAR; WAIT_L(0); MMA_(1, 0, At, B0); MMA_(1, 1, At, B1); BAR; }
    if (wr == 0) BAR;
#undef SA_
#undef SB_
#undef STAGE_A
#undef STAGE_B
#undef LDA_
#undef LDB_
#undef MMA_
}

__global__ __launch_bounds__(256) void transpose_kernel(const bf16_t* __restrict__ W, bf16_t* __restrict__ Wt, int K, int N, int which) {
    __shared__ bf16_t tile[64][72];
    const int nb = N / 64, bk = blockIdx.x / nb, bn = blockIdx.x % nb, t = threadIdx.x;
#pragma unroll
    for (int i = 0; i < 2; ++i) { const int r = (t >> 3) + i * 32, cc = (t & 7) * 8;
        u32x4 v = *(const u32x4*)(W + (size_t)(bk * 64 + r) * N + bn * 64 + cc);
        *(u32x4*)&tile[r][cc] = v; }
    __syncthreads();
#pragma unroll
    for (int i = 0; i < 2; ++i) { const int n = (t >> 3) + i * 32, kc = (t & 7) * 8;
        unsigned short e[8];
#pragma unroll
        for (int j = 0; j < 8; ++j) e[j] = tile[kc + j][n];
        u32x4 v; v.x = e[0] | ((unsigned)e[1] << 16); v.y = e[2] | ((unsigned)e[3] << 16); v.z = e[4] | ((unsigned)e[5] << 16); v.w = e[6] | ((unsigned)e[7] << 16);
        int ng = bn * 64 + n; if (which >= 0) ng = (ng >> 7) * 384 + which * 128 + (ng & 127);
        *(u32x4*)(Wt + (size_t)ng * K + bk * 64 + kc) = v; }
}

__device__ __forceinline__ float wave_sum(float v) {
#pragma unroll
    for (int o = 32; o >= 1; o >>= 1) v += __shfl_xor(v, o);
    return v;
}
__device__ __forceinline__ void ln16(const float (&x)[16], __amdgpu_buffer_rsrc_t rs, unsigned dsto, int lane) {
    float s = 0.f;
#pragma unroll
    for (int j = 0; j < 16; ++j) s += x[j];
    const float mean = wave_sum(s) * (1.0f / 1024.0f);
    float q = 0.f;
#pragma unroll
    for (int j = 0; j < 16; ++j) { const float d = x[j] - mean; q += d * d; }
    const float var = rbf(wave_sum(q) * (1.0f / 1024.0f));
    const float mb = rbf(mean);
    const float sd = rbf(sqrtf(rbf(var + 1e-5f)));
    const float inv = 1.0f / sd;
    float y[16];
#pragma unroll
    for (int j = 0; j < 16; ++j) y[j] = rbf(x[j] - mb) * inv;
    u32x4 a, b;
    a.x = cvt_pk_bf16(y[0], y[1]); a.y = cvt_pk_bf16(y[2], y[3]); a.z = cvt_pk_bf16(y[4], y[5]); a.w = cvt_pk_bf16(y[6], y[7]);
    b.x = cvt_pk_bf16(y[8], y[9]); b.y = cvt_pk_bf16(y[10], y[11]); b.z = cvt_pk_bf16(y[12], y[13]); b.w = cvt_pk_bf16(y[14], y[15]);
    st16_sc1(a, rs, dsto + lane * 16); st16_sc1(b, rs, dsto + 1024 + lane * 16);
}
__device__ __forceinline__ void unpack8(u32x4 v, float* x) { x[0] = bflo(v.x); x[1] = bfhi(v.x); x[2] = bflo(v.y); x[3] = bfhi(v.y); x[4] = bflo(v.z); x[5] = bfhi(v.z); x[6] = bflo(v.w); x[7] = bfhi(v.w); }

__device__ __forceinline__ void attn_task(LAS unsigned char* lds, int seg, int h, const bf16_t* qkv, const bf16_t* kmem, const bf16_t* vmem, __amdgpu_buffer_rsrc_t rs, unsigned ob_off) {
    const int w = threadIdx.x >> 6, lane = threadIdx.x & 63;
    const int fr = lane & 15, g4 = lane >> 4;
    LAS unsigned char* vl = lds + w * 64 * 272;
    const bf16_t* qb = qkv + (size_t)(seg * 32) * 12288 + h * 384;
    const bf16_t* km = kmem + (size_t)(seg * 32 + h) * 1024;
    const bf16_t* vm = vmem + (size_t)(seg * 32 + h) * 1024;
    u32x4 vv[10];
#pragma unroll
    for (int i = 0; i < 10; ++i) { const int key = i * 4 + g4;
        const bf16_t* p = key < 8 ? vm + key * 128 : qb + (size_t)(key - 8) * 12288 + 256;
        vv[i] = *(const u32x4*)(p + fr * 8); }
    bf16x8 qf[4][2], kf[4][3];
#pragma unroll
    for (int kt = 0; kt < 3; ++kt) { int key = kt * 16 + fr; if (key > 39) key = 39; const bf16_t* kr = key < 8 ? km + key * 128 : qb + (size_t)(key - 8) * 12288 + 128;
#pragma unroll
        for (int ks = 0; ks < 4; ++ks) kf[ks][kt] = *(const bf16x8*)(kr + ks * 32 + g4 * 8); }
#pragma unroll
    for (int qt = 0; qt < 2; ++qt)
#pragma unroll
        for (int ks = 0; ks < 4; ++ks) qf[ks][qt] = *(const bf16x8*)(qb + (size_t)(qt * 16 + fr) * 12288 + ks * 32 + g4 * 8);
    __builtin_amdgcn_sched_barrier(0);
#pragma unroll
    for (int i = 0; i < 16; ++i) { const int key = i * 4 + g4;
        u32x4 v = {0u, 0u, 0u, 0u}; if (i < 10) v = vv[i];
        *(LAS u32x4*)(vl + key * 272 + fr * 16) = v; }
    f32x4 sc[3][2];
#pragma unroll
    for (int kt = 0; kt < 3; ++kt)
#pragma unroll
        for (int qt = 0; qt < 2; ++qt) sc[kt][qt] = (f32x4){0.f, 0.f, 0.f, 0.f};
#pragma unroll
    for (int ks = 0; ks < 4; ++ks)
#pragma unroll
        for (int kt = 0; kt < 3; ++kt)
#pragma unroll
            for (int qt = 0; qt < 2; ++qt) sc[kt][qt] = __builtin_amdgcn_mfma_f32_16x16x32_bf16(kf[ks][kt], qf[ks][qt], sc[kt][qt], 0, 0, 0);
    unsigned pk[2][3][2];
#pragma unroll
    for (int qt = 0; qt < 2; ++qt) {
        const int qi = qt * 16 + fr; float sv[3][4]; float m = -3.0e38f;
#pragma unroll
        for (int kt = 0; kt < 3; ++kt)
#pragma unroll
            for (int j = 0; j < 4; ++j) { const int key = kt * 16 + 4 * g4 + j; float v = rbf(rbf(sc[kt][qt][j]) * 0.08838834764831845f);
                const bool ok = key < 8 || (key < 40 && key - 8 <= qi); v = ok ? v : -1.0e9f; sv[kt][j] = v; m = fmaxf(m, v); }
        m = fmaxf(m, __shfl_xor(m, 16)); m = fmaxf(m, __shfl_xor(m, 32));
        float d = 0.f;
#pragma unroll
        for (int kt = 0; kt < 3; ++kt)
#pragma unroll
            for (int j = 0; j < 4; ++j) { const int key = kt * 16 + 4 * g4 + j; float e = key < 40 ? __expf(sv[kt][j] - m) : 0.f; sv[kt][j] = e; d += e; }
        d += __shfl_xor(d, 16); d += __shfl_xor(d, 32);
        const float inv = 1.0f / d;
#pragma unroll
        for (int kt = 0; kt < 3; ++kt) { pk[qt][kt][0] = cvt_pk_bf16(sv[kt][0] * inv, sv[kt][1] * inv); pk[qt][kt][1] = cvt_pk_bf16(sv[kt][2] * inv, sv[kt][3] * inv); }
    }
    __builtin_amdgcn_s_waitcnt(0xc07f);
    const int q4 = (lane & 15) >> 2, p4 = lane & 3;
#pragma unroll
    for (int et = 0; et < 8; ++et) {
        f32x4 oa[2] = {{0.f, 0.f, 0.f, 0.f}, {0.f, 0.f, 0.f, 0.f}};
#pragma unroll
        for (int kst = 0; kst < 2; ++kst) {
            LAS unsigned char* a0 = vl + ((2 * kst) * 16 + 4 * g4 + q4) * 272 + (16 * et + 4 * p4) * 2;
            v4s lo = __builtin_amdgcn_ds_read_tr16_b64_v4i16((LAS v4s*)a0);
            v4s hi = __builtin_amdgcn_ds_read_tr16_b64_v4i16((LAS v4s*)(a0 + 16 * 272));
            bf16x8 vf; vf[0] = lo[0]; vf[1] = lo[1]; vf[2] = lo[2]; vf[3] = lo[3]; vf[4] = hi[0]; vf[5] = hi[1]; vf[6] = hi[2]; vf[7] = hi[3];
#pragma unroll
            for (int qt = 0; qt < 2; ++qt) {
                u32x4 pw; pw.x = pk[qt][2 * kst][0]; pw.y = pk[qt][2 * kst][1];
                if (kst == 0) { pw.z = pk[qt][1][0]; pw.w = pk[qt][1][1]; } else { pw.z = 0u; pw.w = 0u; }
                oa[qt] = __builtin_amdgcn_mfma_f32_16x16x32_bf16(vf, __builtin_bit_cast(bf16x8, pw), oa[qt], 0, 0, 0);
            }
        }
#pragma unroll
        for (int qt = 0; qt < 2; ++qt) { u32x2 ov; ov.x = cvt_pk_bf16(oa[qt][0], oa[qt][1]); ov.y = cvt_pk_bf16(oa[qt][2], oa[qt][3]);
            st8b_sc1(ov, rs, ob_off + (unsigned)((seg * 32 + qt * 16 + fr) * 4096 + h * 128 + et * 16 + 4 * g4) * 2u); }
    }
}

__device__ __forceinline__ float gelu_tanh(float x) {
    const float u = 0.7978845608028654f * (x + 0.044715f * x * x * x);
    const float e = __expf(-2.0f * u);
    return x * __builtin_amdgcn_rcpf(1.0f + e);
}

struct MegaArgs {
    ChainArgs ch;
    char* ws;
    const bf16_t* x; const bf16_t* b1; const bf16_t* b2; bf16_t* out;
    unsigned* queue;
    unsigned o_wqkv, o_wo, o_w1, o_w2, o_xn1, o_xa0, o_xna0, o_xb0, o_xnb0, o_xna1, o_qkv, o_ob, o_hb, o_slab, o_kv, o_xa1;
    int D; int pad1;
};

__device__ __forceinline__ void wait_ctr(unsigned* p, unsigned target, unsigned* tmo) {
    unsigned spins = 0;
    for (;;) {
        const unsigned v = __hip_atomic_load((GAS unsigned*)p, __ATOMIC_RELAXED, __HIP_MEMORY_SCOPE_AGENT);
        if (__builtin_amdgcn_readfirstlane(v) >= target) break;
        __builtin_amdgcn_s_sleep(4);
        if (++spins > WK_CAP) { st4_sc1(tmo, 9u); break; }
        if ((spins & 63u) == 0u && __builtin_amdgcn_readfirstlane(__hip_atomic_load((GAS unsigned*)tmo, __ATOMIC_RELAXED, __HIP_MEMORY_SCOPE_AGENT)) != 0u) break;
    }
}

template <int MODE>
__device__ __forceinline__ void gemm_task(LAS unsigned char* lds, const bf16_t* A, int lda, const bf16_t* Bt, int ldb, int nt, __amdgpu_buffer_rsrc_t rs, unsigned c_off, int ldc, const bf16_t* bias) {
    f32x4 acc[2][2][2][2];
    gemm_tile<2, MODE != 2>(lds, A, lda, Bt, ldb, nt, acc);
    const int tid = threadIdx.x, wid = tid >> 6, lane = tid & 63, wr = wid >> 2, wc = wid & 3, fr = lane & 15, fq = lane >> 4;
    const int row0 = wr * 32 + fr;
    if constexpr (MODE == 2) {
        const unsigned col0 = wc * 32 + 4 * fq;
#pragma unroll
        for (int ai = 0; ai < 2; ++ai)
#pragma unroll
            for (int m = 0; m < 2; ++m) { const unsigned ro = c_off + ((unsigned)(row0 + ai * 64 + m * 16) * ldc + col0) * 4u;
#pragma unroll
                for (int bj = 0; bj < 2; ++bj)
#pragma unroll
                    for (int n = 0; n < 2; ++n) st16_sc1(__builtin_bit_cast(u32x4, acc[ai][bj][m][n]), rs, ro + (bj * 128 + n * 16) * 4); }
    } else {
        const unsigned col0 = wc * 32 + 8 * fq;
        float bv[2][8];
        if constexpr (MODE == 1) {
#pragma unroll
            for (int bj = 0; bj < 2; ++bj) { u32x4 b = *(const u32x4*)(bias + col0 + bj * 128);
                bv[bj][0] = bflo(b.x); bv[bj][1] = bfhi(b.x); bv[bj][2] = bflo(b.y); bv[bj][3] = bfhi(b.y); bv[bj][4] = bflo(b.z); bv[bj][5] = bfhi(b.z); bv[bj][6] = bflo(b.w); bv[bj][7] = bfhi(b.w); }
        }
#pragma unroll
        for (int ai = 0; ai < 2; ++ai)
#pragma unroll
            for (int m = 0; m < 2; ++m) { const unsigned ro = c_off + ((unsigned)(row0 + ai * 64 + m * 16) * ldc + col0) * 2u;
#pragma unroll
                for (int bj = 0; bj < 2; ++bj) { f32x4 v0 = acc[ai][bj][m][0], v1 = acc[ai][bj][m][1];
                    if constexpr (MODE == 1) {
#pragma unroll
                        for (int j = 0; j < 4; ++j) { v0[j] = gelu_tanh(rbf(rbf(v0[j]) + bv[bj][j])); v1[j] = gelu_tanh(rbf(rbf(v1[j]) + bv[bj][4 + j])); }
                    }
                    u32x4 w; w.x = cvt_pk_bf16(v0[0], v0[1]); w.y = cvt_pk_bf16(v0[2], v0[3]); w.z = cvt_pk_bf16(v1[0], v1[1]); w.w = cvt_pk_bf16(v1[2], v1[3]);
                    st16_sc1(w, rs, ro + bj * 256); } }
    }
}

__device__ __forceinline__ void red_task(const float* slabs, int nslab, const bf16_t* bias, const bf16_t* xi, bf16_t* xo_plain, __amdgpu_buffer_rsrc_t rs, unsigned xo_off, unsigned xn_off, bool do_ln, int row) {
    const int lane = threadIdx.x & 63;
    float acc[16];
#pragma unroll
    for (int j = 0; j < 16; ++j) acc[j] = 0.f;
    {
        f32x4 sv[8][4];
#pragma unroll
        for (int sidx = 0; sidx < 8; ++sidx) {
            const float* p = slabs + (size_t)(sidx < nslab ? sidx : 0) * ((size_t)S_ * DM) + (size_t)row * DM + lane * 8;
            sv[sidx][0] = *(const f32x4*)p; sv[sidx][1] = *(const f32x4*)(p + 4); sv[sidx][2] = *(const f32x4*)(p + 512); sv[sidx][3] = *(const f32x4*)(p + 516);
        }
        __builtin_amdgcn_sched_barrier(0);
#pragma unroll
        for (int sidx = 0; sidx < 8; ++sidx) if (sidx < nslab) {
#pragma unroll
            for (int q = 0; q < 4; ++q)
#pragma unroll
                for (int j = 0; j < 4; ++j) acc[q * 4 + j] += sv[sidx][q][j];
        }
    }
    float xv[16], bv[16];
    unpack8(*(const u32x4*)(xi + (size_t)row * DM + lane * 8), xv); unpack8(*(const u32x4*)(xi + (size_t)row * DM + 512 + lane * 8), xv + 8);
    if (bias) { unpack8(*(const u32x4*)(bias + lane * 8), bv); unpack8(*(const u32x4*)(bias + 512 + lane * 8), bv + 8); }
#pragma unroll
    for (int j = 0; j < 16; ++j) { float t = rbf(acc[j]); if (bias) t = rbf(t + bv[j]); xv[j] = rbf(xv[j] + t); }
    u32x4 a, b;
    a.x = cvt_pk_bf16(xv[0], xv[1]); a.y = cvt_pk_bf16(xv[2], xv[3]); a.z = cvt_pk_bf16(xv[4], xv[5]); a.w = cvt_pk_bf16(xv[6], xv[7]);
    b.x = cvt_pk_bf16(xv[8], xv[9]); b.y = cvt_pk_bf16(xv[10], xv[11]); b.z = cvt_pk_bf16(xv[12], xv[13]); b.w = cvt_pk_bf16(xv[14], xv[15]);
    if (xo_plain) { *(u32x4*)(xo_plain + (size_t)row * DM + lane * 8) = a; *(u32x4*)(xo_plain + (size_t)row * DM + 512 + lane * 8) = b; }
    else { st16_sc1(a, rs, xo_off + (unsigned)row * 2048u + lane * 16); st16_sc1(b, rs, xo_off + (unsigned)row * 2048u + 1024 + lane * 16); }
    if (do_ln) ln16(xv, rs, xn_off + (unsigned)row * 2048u, lane);
}

#define WSTAMP(i) do {} while (0)
constexpr int NTASK = 5440;
__device__ __forceinline__ void worker_role(const MegaArgs& a, LAS unsigned char* lds) {
    const int tid = threadIdx.x, wid = __builtin_amdgcn_readfirstlane(tid >> 6);
    __amdgpu_buffer_rsrc_t rs = __builtin_amdgcn_make_buffer_rsrc((void*)a.ws, 0, 0x10000000, 0x00020000);
    LAS unsigned* sidx = (LAS unsigned*)(lds + 159 * 1024);
    unsigned* ctr = a.ch.ctr; unsigned* tmo = a.ch.tmo;
    const int D = a.D;
#pragma unroll 1
    for (;;) {
        if (wid == 0) { const unsigned old = __hip_atomic_fetch_add((GAS unsigned*)a.queue, (tid & 63) == 0 ? 1u : 0u, __ATOMIC_RELAXED, __HIP_MEMORY_SCOPE_AGENT);
            *sidx = __builtin_amdgcn_readfirstlane(old); }
        __syncthreads();
        int idx = __builtin_amdgcn_readfirstlane(*sidx);
        const int qidx = idx; (void)qidx;
        WSTAMP(0);
        __syncthreads();
        if (idx >= NTASK) break;
        int ph, l, b, sub;
        if (idx < 768) { ph = PH_QKV; l = 0; b = idx / 48; sub = idx % 48; }
        else {
            if (wid == 0) {
                unsigned spins = 0, d;
                for (;;) {
                    d = __builtin_amdgcn_readfirstlane(__hip_atomic_load((GAS unsigned*)(a.ch.slots + (idx - 768) * 16), __ATOMIC_RELAXED, __HIP_MEMORY_SCOPE_AGENT));
                    if (d != 0u) break;
                    __builtin_amdgcn_s_sleep(24);
                    if (++spins > WK_CAP) { st4_sc1(tmo, 9u); break; }
                    if ((spins & 63u) == 0u && __builtin_amdgcn_readfirstlane(__hip_atomic_load((GAS unsigned*)tmo, __ATOMIC_RELAXED, __HIP_MEMORY_SCOPE_AGENT)) != 0u) break;
                }
                *sidx = d;
                __builtin_amdgcn_fence(__ATOMIC_ACQUIRE, "agent");
                asm volatile("s_waitcnt vmcnt(0)" ::: "memory");
            }
            __syncthreads();
            unsigned d = __builtin_amdgcn_readfirstlane(*sidx);
            if (d == 0u) break;
            d -= 1u; ph = d & 7; l = (d >> 3) & 1; b = (d >> 4) & 15; sub = d >> 8;
        }
        WSTAMP(1);
        const char* ws = a.ws;
        if (ph == PH_QKV) {
            const bf16_t* A = (const bf16_t*)(ws + (l ? a.o_xnb0 : a.o_xn1)) + (size_t)b * 128 * 1024;
            gemm_task<0>(lds, A, 1024, (const bf16_t*)(ws + a.o_wqkv) + (size_t)sub * 256 * 1024, 1024, 16, rs, a.o_qkv + ((unsigned)b * 128u * 12288u + sub * 256u) * 2u, 12288, nullptr);
        } else if (ph == PH_ATT) {
            const int task = sub * 8 + wid, seg = b * 4 + (task >> 5), h = task & 31;
            const bf16_t* km = (const bf16_t*)(ws + a.o_kv) + (size_t)(l * 2) * 64 * 32 * 1024;
            attn_task(lds, seg, h, (const bf16_t*)(ws + a.o_qkv), km, km + (size_t)64 * 32 * 1024, rs, a.o_ob);
        } else if (ph == PH_OUT) {
            const int pn = sub & 3, ks = sub >> 2;
            gemm_task<2>(lds, (const bf16_t*)(ws + a.o_ob) + (size_t)b * 128 * 4096 + ks * 512, 4096, (const bf16_t*)(ws + a.o_wo) + (size_t)pn * 256 * 4096 + ks * 512, 4096, 8, rs,
                         a.o_slab + ((unsigned)ks * (S_ * DM) + (unsigned)b * 128u * 1024u + pn * 256u) * 4u, 1024, nullptr);
        } else if (ph == PH_REDA) {
            const int row = b * 128 + sub * 8 + wid;
            red_task((const float*)(ws + a.o_slab), 8, nullptr, l ? (const bf16_t*)(ws + a.o_xb0) : a.x, nullptr, rs, l ? a.o_xa1 : a.o_xa0, l ? a.o_xna1 : a.o_xna0, true, row);
        } else if (ph == PH_FFN1) {
            gemm_task<1>(lds, (const bf16_t*)(ws + (l ? a.o_xna1 : a.o_xna0)) + (size_t)b * 128 * 1024, 1024, (const bf16_t*)(ws + a.o_w1) + (size_t)sub * 256 * 1024, 1024, 16, rs,
                         a.o_hb + ((unsigned)b * 128u * FF + sub * 256u) * 2u, FF, a.b1 + sub * 256);
        } else if (ph == PH_FFN2) {
            const int pn = sub & 3, ks = sub >> 2;
            gemm_task<2>(lds, (const bf16_t*)(ws + a.o_hb) + (size_t)b * 128 * FF + ks * 512, FF, (const bf16_t*)(ws + a.o_w2) + (size_t)pn * 256 * FF + ks * 512, FF, 8, rs,
                         a.o_slab + ((unsigned)ks * (S_ * DM) + (unsigned)b * 128u * 1024u + pn * 256u) * 4u, 1024, nullptr);
        } else {
            const int row = b * 128 + sub * 8 + wid;
            if (l == 0) red_task((const float*)(ws + a.o_slab), 7, a.b2, (const bf16_t*)(ws + a.o_xa0), nullptr, rs, a.o_xb0, a.o_xnb0, true, row);
            else red_task((const float*)(ws + a.o_slab), 7, a.b2, (const bf16_t*)(ws + a.o_xa1), a.out, rs, 0, 0, false, row);
        }
        WSTAMP(2);
        asm volatile("s_waitcnt vmcnt(0)" ::: "memory");
        __syncthreads();
        WSTAMP(3);
#define CTR(L_, PH, B_) (ctr + (((L_) * 8 + (PH)) * 16 + (B_)) * 64)
        if (wid == 0) {
            const int lane = tid & 63;
            if (ph == PH_QKV) {
                wave_add_ret(CTR(l, PH_QKV, b), 1u, lane);
            } else if (wave_add_ret(CTR(l, ph, b), 1u, lane) == (unsigned)ph_count(ph) - 1u) {
                if (ph < PH_REDB) push_group(a.ch.slots, a.ch.tail, ph + 1, l, b, ph_count(ph + 1), lane);
                else if (l == 0) push_group(a.ch.slots, a.ch.tail, PH_QKV, 1, b, 48, lane);
            }
        }
#undef CTR
    }
}

__global__ __launch_bounds__(512, 2) void mega_kernel(MegaArgs a) {
    extern __shared__ __attribute__((aligned(16))) unsigned char shm[];
    LAS unsigned char* lds = (LAS unsigned char*)shm;
    if (blockIdx.x < 64) chain_role(a.ch, lds, blockIdx.x); else worker_role(a, lds);
}

__global__ __launch_bounds__(256) void ln_kernel(const bf16_t* __restrict__ x, char* ws, unsigned xn_off) {
    const int row = blockIdx.x * 4 + (threadIdx.x >> 6), lane = threadIdx.x & 63;
    __amdgpu_buffer_rsrc_t rs = __builtin_amdgcn_make_buffer_rsrc((void*)ws, 0, 0x10000000, 0x00020000);
    float v[16];
    unpack8(*(const u32x4*)(x + (size_t)row * DM + lane * 8), v); unpack8(*(const u32x4*)(x + (size_t)row * DM + 512 + lane * 8), v + 8);
    ln16(v, rs, xn_off + (unsigned)row * 2048u, lane);
}

#define LAG 20
extern "C" void kernel_launch(void* const* d_in, const int* in_sizes, int n_in, void* d_out, int out_size, void* d_ws, size_t ws_size, hipStream_t stream) {
    const bf16_t* x = (const bf16_t*)d_in[0]; const bf16_t* mem0 = (const bf16_t*)d_in[1];
    const bf16_t* Wq = (const bf16_t*)d_in[2]; const bf16_t* Wk = (const bf16_t*)d_in[3]; const bf16_t* Wv = (const bf16_t*)d_in[4]; const bf16_t* Wo = (const bf16_t*)d_in[5];
    const bf16_t* w1 = (const bf16_t*)d_in[8]; const bf16_t* b1 = (const bf16_t*)d_in[9]; const bf16_t* w2 = (const bf16_t*)d_in[10]; const bf16_t* b2 = (const bf16_t*)d_in[11];
    char* ws = (char*)d_ws; size_t off = 0;
    auto alloc = [&](size_t bytes) { size_t p = off; off += (bytes + 255) & ~(size_t)255; return p; };
    const int T = 64 + LAG;
    const size_t o_ctr = alloc(2 * 8 * 16 * 256), o_queue = alloc(256), o_tmo = alloc(256), o_tail = alloc(256), o_slots = alloc(5440 * 64);
    const size_t zero_bytes = off;
    const size_t o_mem = alloc((size_t)(T + 1) * 32768), o_U = alloc(64 * 32768), o_X = alloc(64 * 8192);
    const size_t sent_bytes = off - zero_bytes;
    const size_t o_kv = alloc((size_t)4 * 64 * 32 * 1024 * 2);
    const size_t o_wqkv = alloc((size_t)12288 * 1024 * 2), o_wo = alloc((size_t)1024 * 4096 * 2), o_w1 = alloc((size_t)FF * 1024 * 2), o_w2 = alloc((size_t)1024 * FF * 2);
    const size_t XB = (size_t)S_ * DM * 2;
    const size_t o_xn1 = alloc(XB), o_xa0 = alloc(XB), o_xna0 = alloc(XB), o_xb0 = alloc(XB), o_xnb0 = alloc(XB), o_xna1 = alloc(XB), o_xa1 = alloc(XB);
    const size_t o_qkv = alloc((size_t)S_ * 12288 * 2), o_ob = alloc((size_t)S_ * 4096 * 2), o_hb = alloc((size_t)S_ * FF * 2);
    const size_t o_slab = alloc((size_t)8 * S_ * DM * 4);

    hipMemsetAsync(ws, 0, zero_bytes, stream);
    hipMemsetAsync(ws + zero_bytes, 0xFF, sent_bytes, stream);
    MegaArgs a;
    a.ch.mem0 = mem0; a.ch.Wq = Wq; a.ch.Wk = Wk; a.ch.Wv = Wv; a.ch.Wo = Wo; a.ch.qkv = (const bf16_t*)(ws + o_qkv);
    a.ch.membuf = (unsigned*)(ws + o_mem); a.ch.Ubuf = (unsigned*)(ws + o_U); a.ch.Xbuf = (unsigned*)(ws + o_X); a.ch.kvmem = (bf16_t*)(ws + o_kv);
    a.ch.tmo = (unsigned*)(ws + o_tmo); a.ch.ctr = (unsigned*)(ws + o_ctr); a.ch.slots = (unsigned*)(ws + o_slots); a.ch.tail = (unsigned*)(ws + o_tail); a.ch.lag = LAG; a.ch.nstep = T; a.ch.qstride = 0; a.ch.pad0 = 0;
    a.ws = ws; a.x = x; a.b1 = b1; a.b2 = b2; a.out = (bf16_t*)d_out; a.queue = (unsigned*)(ws + o_queue);
    a.o_wqkv = o_wqkv; a.o_wo = o_wo; a.o_w1 = o_w1; a.o_w2 = o_w2; a.o_xn1 = o_xn1; a.o_xa0 = o_xa0; a.o_xna0 = o_xna0; a.o_xb0 = o_xb0; a.o_xnb0 = o_xnb0; a.o_xna1 = o_xna1;
    a.o_qkv = o_qkv; a.o_ob = o_ob; a.o_hb = o_hb; a.o_slab = o_slab; a.o_kv = o_kv; a.o_xa1 = o_xa1; a.D = 1 + LAG / 4; a.pad1 = 0;

    bf16_t* Wqkv_t = (bf16_t*)(ws + o_wqkv);
    transpose_kernel<<<16 * 64, 256, 0, stream>>>(Wq, Wqkv_t, 1024, 4096, 0);
    transpose_kernel<<<16 * 64, 256, 0, stream>>>(Wk, Wqkv_t, 1024, 4096, 1);
    transpose_kernel<<<16 * 64, 256, 0, stream>>>(Wv, Wqkv_t, 1024, 4096, 2);
    transpose_kernel<<<64 * 16, 256, 0, stream>>>(Wo, (bf16_t*)(ws + o_wo), 4096, 1024, -1);
    transpose_kernel<<<16 * 56, 256, 0, stream>>>(w1, (bf16_t*)(ws + o_w1), 1024, FF, -1);
    transpose_kernel<<<56 * 16, 256, 0, stream>>>(w2, (bf16_t*)(ws + o_w2), FF, 1024, -1);
    ln_kernel<<<S_ / 4, 256, 0, stream>>>(x, ws, (unsigned)o_xn1);
    hipFuncSetAttribute((const void*)mega_kernel, hipFuncAttributeMaxDynamicSharedMemorySize, CH_LDS);
    mega_kernel<<<256, 512, CH_LDS, stream>>>(a);
}
```

```cpp
#include <hip/hip_runtime.h>
#include <stdint.h>
#define LAS __attribute__((address_space(3)))
#define GAS __attribute__((address_space(1)))
typedef unsigned short bf16_t;
typedef short bf16x8 __attribute__((ext_vector_type(8)));
typedef short bf16x4 __attribute__((ext_vector_type(4)));
typedef float f32x4 __attribute__((ext_vector_type(4)));
typedef float f32x2 __attribute__((ext_vector_type(2)));
typedef unsigned u32x4 __attribute__((ext_vector_type(4)));
typedef unsigned u32x2 __attribute__((ext_vector_type(2)));
typedef __bf16 bf16v2 __attribute__((ext_vector_type(2)));

__device__ __forceinline__ unsigned cvt_pk_bf16(float lo, float hi) { f32x2 v = {lo, hi}; bf16v2 r = __builtin_convertvector(v, bf16v2); return __builtin_bit_cast(unsigned, r); }
__device__ __forceinline__ float rbf(float x) { return __uint_as_float(cvt_pk_bf16(x, x) << 16); }
__device__ __forceinline__ float bflo(unsigned w) { return __uint_as_float(w << 16); }
__device__ __forceinline__ float bfhi(unsigned w) { return __uint_as_float(w & 0xFFFF0000u); }

constexpr int S_ = 2048, DM = 1024, NH = 32, DH = 128, NSEG = 64, FF = 3584;
constexpr unsigned SENT = 0xFFFFFFFFu;

struct ChainArgs {
    const bf16_t* mem0; const bf16_t* Wq; const bf16_t* Wk; const bf16_t* Wv; const bf16_t* Wo;
    char* wsb;
    unsigned o_qkv, o_mem, o_U, o_X, o_kv, o_zero;
    unsigned* tmo;
    unsigned* ctr;
    int lag; int nstep;
    unsigned qstride; unsigned pad0;
};
#define STAMP(i) do {} while (0)
template <int N_> __device__ __forceinline__ void wait_vm() { asm volatile("s_waitcnt vmcnt(%0)" :: "n"(N_) : "memory"); }
#define LBAR() do { asm volatile("s_waitcnt lgkmcnt(0)" ::: "memory"); __builtin_amdgcn_s_barrier(); asm volatile("" ::: "memory"); } while (0)
#define CH_CAP 100000u
#define WK_CAP 20000u
#define CH_WL 0
#define CH_OV (128*1024)
#define CH_LDS (160*1024)

__device__ __forceinline__ u32x4 ld16_sc1(__amdgpu_buffer_rsrc_t r, unsigned off) { asm volatile("" ::: "memory"); u32x4 v = __builtin_amdgcn_raw_buffer_load_b128(r, off, 0, 16); asm volatile("" ::: "memory"); return v; }
__device__ __forceinline__ void st16_sc1(u32x4 v, __amdgpu_buffer_rsrc_t r, unsigned off) { __builtin_amdgcn_raw_buffer_store_b128(v, r, off, 0, 16); }
__device__ __forceinline__ void st8b_sc1(u32x2 v, __amdgpu_buffer_rsrc_t r, unsigned off) { __builtin_amdgcn_raw_buffer_store_b64(v, r, off, 0, 16); }
__device__ __forceinline__ void st8_sc1(void* p, u32x2 v) { __hip_atomic_store((GAS unsigned long long*)p, ((unsigned long long)v.y << 32) | v.x, __ATOMIC_RELAXED, __HIP_MEMORY_SCOPE_AGENT); }
__device__ __forceinline__ void st4_sc1(unsigned* p, unsigned v) { __hip_atomic_store((GAS unsigned*)p, v, __ATOMIC_RELAXED, __HIP_MEMORY_SCOPE_AGENT); }

enum { PH_QKV = 0, PH_ATT = 1, PH_OUT = 2, PH_REDA = 3, PH_FFN1 = 4, PH_FFN2 = 5, PH_REDB = 6, PH_ARDY = 7 };
#define X1 2
#define G0 16
#define U0 14
#define WPRE 5
#define KXA 0
#define VXA 16
#define NCOPY 1
#define XSTR 16384
#define SPB 2
#define NBLK (64 / SPB)
#define RG (2 * SPB)
__host__ __device__ __forceinline__ constexpr int ph_count(int ph) { return ph == PH_QKV ? 96 : ph == PH_ATT ? 8 * SPB : ph == PH_OUT ? 32 : ph == PH_REDA ? 4 * SPB : ph == PH_FFN1 ? 28 : ph == PH_FFN2 ? 32 : ph == PH_REDB ? 4 * SPB : 64; }
#define KVR 8
#define FSTR 16
#define FGRP 296
#define FLG(L_, PH, B_) (((L_) * NBLK + (B_)) * FGRP + (int)((0x1C1B1713120E0C00ull >> (8 * (PH))) & 0xFFull) * 8)
#define FLG_INIT (2 * NBLK * FGRP)
#define FLG_LN (FLG_INIT + 64)
#define FLG_SLOTS (FLG_LN + 96)
typedef short v4s __attribute__((ext_vector_type(4)));
__device__ __forceinline__ bf16x8 tr_frag_at(LAS unsigned char* a0) {
    v4s lo = __builtin_amdgcn_ds_read_tr16_b64_v4i16((LAS v4s*)a0);
    v4s hi = __builtin_amdgcn_ds_read_tr16_b64_v4i16((LAS v4s*)(a0 + 4 * 256));
    bf16x8 r; r[0] = lo[0]; r[1] = lo[1]; r[2] = lo[2]; r[3] = lo[3]; r[4] = hi[0]; r[5] = hi[1]; r[6] = hi[2]; r[7] = hi[3];
    return r;
}
template <int CTRL> __device__ __forceinline__ float dpp_f(float x) { return __int_as_float(__builtin_amdgcn_update_dpp(0, __float_as_int(x), CTRL, 0xF, 0xF, false)); }
__device__ __forceinline__ float row16_max(float x) { x = fmaxf(x, dpp_f<0x128>(x)); x = fmaxf(x, dpp_f<0x124>(x)); x = fmaxf(x, dpp_f<0x122>(x)); x = fmaxf(x, dpp_f<0x121>(x)); return x; }
__device__ __forceinline__ float row16_sum(float x) { x += dpp_f<0x128>(x); x += dpp_f<0x124>(x); x += dpp_f<0x122>(x); x += dpp_f<0x121>(x); return x; }
__device__ __forceinline__ unsigned or4x(u32x4 v, unsigned m) { return (v.x ^ m) | (v.y ^ m) | (v.z ^ m) | (v.w ^ m); }
__device__ __forceinline__ u32x4 tag4(f32x4 s, unsigned tag) { return (u32x4){(__float_as_uint(s[0]) & ~1u) | tag, (__float_as_uint(s[1]) & ~1u) | tag, (__float_as_uint(s[2]) & ~1u) | tag, (__float_as_uint(s[3]) & ~1u) | tag}; }
__device__ __forceinline__ float untag(unsigned x) { return __uint_as_float(x & ~1u); }

#define OV_V1 10880
#define OV_P 21760
#define OV_O 24576

__device__ __forceinline__ void chain_role(const ChainArgs& a, LAS unsigned char* lds, int c) {
    const int tid = threadIdx.x, w = __builtin_amdgcn_readfirstlane(tid >> 6);
    const int h = c >> 1, hf = c & 1, kh = w >> 2;
    bf16x8 wqk[32], wv[16], wo0;
    {
        const int lane = tid & 63;
        u32x2 m0 = {0u, 0u};
        if (w == 0) m0 = *(const u32x2*)(a.mem0 + (lane & 7) * 1024 + c * 16 + (lane >> 4) * 4);
        const int g = lane >> 4, q = (lane & 15) >> 2, p = lane & 3;
        const int row = tid >> 4, ch = tid & 15;
        const bf16_t* sqk = (ch < 8 ? a.Wq : a.Wk) + (size_t)row * 4096 + h * 128 + hf * 64 + (ch & 7) * 8;
        const bf16_t* sv = a.Wv + (size_t)((ch >> 3) * 512 + row) * 4096 + h * 128 + hf * 64 + (ch & 7) * 8;
        const bf16_t* so = a.Wo + (size_t)(h * 128 + row) * 1024 + hf * 512 + ch * 8;
#define CH_ISSUE(k_) do { _Pragma("unroll") for (int i = 0; i < 8; ++i) { \
            const bf16_t* src = (k_) < 4 ? sqk + (size_t)((k_) * 8 + i) * 32 * 4096 : (k_) < 6 ? sv + (size_t)(((k_) - 4) * 8 + i) * 32 * 4096 : so + (((k_) - 6) * 2 + (i >> 2)) * 128 + (size_t)(i & 3) * 32 * 1024; \
            __builtin_amdgcn_global_load_lds((const unsigned*)src, (LAS unsigned*)(lds + ((k_) & 1) * 65536 + i * 8192 + w * 1024), 16, 0, 0); } } while (0)
        CH_ISSUE(0); CH_ISSUE(1);
#pragma unroll
        for (int k = 0; k < 8; ++k) {
            if (k == 2 && w == 0) { wait_vm<0>(); if (lane == 0) st4_sc1(a.ctr + (FLG_INIT + c) * FSTR, 1u); }
            if (k < 7) asm volatile("s_waitcnt vmcnt(8)" ::: "memory"); else asm volatile("s_waitcnt vmcnt(0)" ::: "memory");
            if (k == 0 && w == 0) {
                const int tok = lane & 15, g4 = lane >> 4;
                if (m0.x == SENT) m0.x = 0x7FC07FC0u; if (m0.y == SENT) m0.y = 0x7FC07FC0u;
                st8_sc1((void*)(a.wsb + a.o_mem + c * 512 + tok * 32 + g4 * 8), m0);
            }
            __syncthreads();
            LAS unsigned char* cb = lds + (k & 1) * 65536 + (8 * g + q) * 256;
            if (k < 4) {
                LAS unsigned char* a0 = cb + ((p < 2 ? 8 * w + 4 * p : 64 + 8 * w + 4 * (p - 2))) * 2;
                if ((k >> 1) == kh) {
#pragma unroll
                    for (int i = 0; i < 8; ++i) wqk[(k & 1) * 8 + i] = tr_frag_at(a0 + i * 8192);
                } else {
#pragma unroll
                    for (int i = 0; i < 8; ++i) wqk[16 + (k & 1) * 8 + i] = tr_frag_at(a0 + i * 8192);
                }
                asm volatile("s_waitcnt lgkmcnt(0)" ::: "memory");
                __syncthreads();
            } else if (k < 6) {
                LAS unsigned char* a0 = cb + (64 * kh + 16 * (w & 3) + 4 * p) * 2;
#pragma unroll
                for (int i = 0; i < 8; ++i) wv[(k - 4) * 8 + i] = tr_frag_at(a0 + i * 8192);
                asm volatile("s_waitcnt lgkmcnt(0)" ::: "memory");
                __syncthreads();
            } else {
                LAS unsigned char* a0 = cb + (16 * w + 4 * p) * 2;
                bf16x8 f[8];
#pragma unroll
                for (int i = 0; i < 8; ++i) f[i] = tr_frag_at(a0 + i * 8192);
                asm volatile("s_waitcnt lgkmcnt(0)" ::: "memory");
                __syncthreads();
#pragma unroll
                for (int i = 0; i < 8; ++i) {
                    if (k == 6 && i == 0) { wo0 = f[0]; continue; }
                    *(LAS bf16x8*)(lds + CH_WL + ((k - 6) * 2 + (i >> 2)) * 32768 + w * 4096 + (i & 3) * 1024 + lane * 16) = f[i]; }
            }
            if (k + 2 < 8) CH_ISSUE(k + 2);
        }
        asm volatile("s_waitcnt lgkmcnt(0)" ::: "memory");
        __syncthreads();
    }
    const int T = a.nstep, L = a.lag;
    __amdgpu_buffer_rsrc_t rs = __builtin_amdgcn_make_buffer_rsrc((void*)a.wsb, 0, 0x10000000, 0x00020000);
    LAS unsigned char* ov = lds + CH_OV;
    bool dead = false, pre_ok = false, kx_ok = false;
    u32x4 kx[2] = {{0u, 0u, 0u, 0u}, {0u, 0u, 0u, 0u}};
#pragma unroll 1
    for (int t = 0; t < T; ++t) {
        int lane = tid & 63; asm volatile("" : "+v"(lane));
        unsigned OOB = 0x80000000u, QNAN = 0x7FC07FC0u; float NEGB = -3.0e38f; asm volatile("" : "+s"(OOB), "+s"(QNAN), "+s"(NEGB));
        const unsigned o_ctr = (unsigned)((const char*)a.ctr - a.wsb);
        int tok = lane & 15, g4 = lane >> 4, slot = tok >> 3;
        const int s0 = t < 64 ? t : 63, s1 = t - L < 0 ? 0 : (t - L > 63 ? 63 : t - L);
        const bool act0 = t < 64, act1 = t >= L && t - L < 64;
        const int sseg = slot ? s1 : s0; const bool act = slot ? act1 : act0;
        const unsigned tag0 = t & 1, tag1 = (t - L) & 1; unsigned tag = slot ? tag1 : tag0;
#define RELANE() do { asm volatile("" : "+v"(lane)); tok = lane & 15; g4 = lane >> 4; slot = tok >> 3; tag = slot ? tag1 : tag0; } while (0)
        if (!pre_ok) {
            unsigned spins = 0;
            for (;;) {
                unsigned v0 = 1u, v1 = 1u, v2 = 1u, v3 = 1u;
                const int u = t - L;
                if (t < 64 && (t % SPB) == 0) { v0 = __hip_atomic_load((GAS unsigned*)(a.ctr + (FLG(0, PH_QKV, t / SPB) + lane) * FSTR), __ATOMIC_RELAXED, __HIP_MEMORY_SCOPE_AGENT);
                    if (t >= KVR && lane < 8 * SPB) v1 = __hip_atomic_load((GAS unsigned*)(a.ctr + (FLG(0, PH_ATT, (t - KVR) / SPB) + lane) * FSTR), __ATOMIC_RELAXED, __HIP_MEMORY_SCOPE_AGENT); }
                if (u >= 0 && u < 64 && (u % SPB) == 0) { v2 = __hip_atomic_load((GAS unsigned*)(a.ctr + (FLG(1, PH_QKV, u / SPB) + lane) * FSTR), __ATOMIC_RELAXED, __HIP_MEMORY_SCOPE_AGENT);
                    if (u >= KVR && lane < 8 * SPB) v3 = __hip_atomic_load((GAS unsigned*)(a.ctr + (FLG(1, PH_ATT, (u - KVR) / SPB) + lane) * FSTR), __ATOMIC_RELAXED, __HIP_MEMORY_SCOPE_AGENT); }
                if (t == 0) v1 = __hip_atomic_load((GAS unsigned*)(a.ctr + (FLG_INIT + lane) * FSTR), __ATOMIC_RELAXED, __HIP_MEMORY_SCOPE_AGENT);
                if (__all(v0 != 0u && v1 != 0u && v2 != 0u && v3 != 0u)) break;
                if (dead || ++spins > CH_CAP) { dead = true; if (lane == 0) st4_sc1(a.tmo, (unsigned)t * 8u + 5u); break; }
            }
            asm volatile("" ::: "memory");
        }
        unsigned f0, f1, f2, f3;
#define CH_FCOND() const int _t1 = t + 1, _u1 = _t1 - L; \
        const bool fc0 = _t1 < 64 && (_t1 % SPB) == 0, fc1 = fc0 && _t1 >= KVR && lane < 8 * SPB, fc2 = _u1 >= 0 && _u1 < 64 && (_u1 % SPB) == 0, fc3 = fc2 && _u1 >= KVR && lane < 8 * SPB
        {   CH_FCOND();
            f0 = __builtin_amdgcn_raw_buffer_load_b32(rs, fc0 ? o_ctr + (unsigned)(FLG(0, PH_QKV, _t1 / SPB) + lane) * (4u * FSTR) : OOB, 0, 16);
            f1 = __builtin_amdgcn_raw_buffer_load_b32(rs, fc1 ? o_ctr + (unsigned)(FLG(0, PH_ATT, (_t1 - KVR) / SPB) + lane) * (4u * FSTR) : OOB, 0, 16);
            f2 = __builtin_amdgcn_raw_buffer_load_b32(rs, fc2 ? o_ctr + (unsigned)(FLG(1, PH_QKV, _u1 / SPB) + lane) * (4u * FSTR) : OOB, 0, 16);
            f3 = __builtin_amdgcn_raw_buffer_load_b32(rs, fc3 ? o_ctr + (unsigned)(FLG(1, PH_ATT, (_u1 - KVR) / SPB) + lane) * (4u * FSTR) : OOB, 0, 16); }
        if (c == 0 && w == 1 && act0 && (s0 % SPB) == SPB - 1 && lane == 0) __builtin_amdgcn_raw_buffer_store_b32(1u, rs, 0, o_ctr + (unsigned)(FLG(0, PH_ARDY, s0 / SPB) + 64) * (4u * FSTR), 16);
        const unsigned qo0 = act0 ? (unsigned)s0 * 786432u + h * 768 + a.o_qkv : OOB, qo1 = act1 ? (unsigned)s1 * 786432u + a.qstride + h * 768 + a.o_qkv : OOB;
#define KX_LOAD(q0_, q1_) do { const unsigned kp = (w < 4 ? ((w >> 1) ? (q1_) : (q0_)) : OOB) + (16 * (w & 1) + tok) * 24576 + 256 + hf * 128 + g4 * 16; \
            kx[0] = __builtin_amdgcn_raw_buffer_load_b128(rs, kp, 0, KXA); kx[1] = __builtin_amdgcn_raw_buffer_load_b128(rs, kp + 64, 0, KXA); } while (0)
        if (!kx_ok) KX_LOAD(qo0, qo1);
        u32x4 vx0, vx1;
        {   const unsigned vp = (kh ? qo1 : qo0) + ((w & 3) * 8 + (lane >> 3)) * 24576 + 512 + (lane & 7) * 32;
            vx0 = __builtin_amdgcn_raw_buffer_load_b128(rs, vp, 0, VXA); vx1 = __builtin_amdgcn_raw_buffer_load_b128(rs, vp + 16, 0, VXA); }
        {
            u32x4 v[4];
            const unsigned gmo = (((lane & 16) ? act1 : act0) ? (unsigned)(t * 32768 + w * 4096) + a.o_mem : OOB) + (unsigned)lane * 16u;
            if (G0) __builtin_amdgcn_s_sleep(G0);
            unsigned spins = 0;
            for (;;) {
                bool ok = true;
#pragma unroll
                for (int i = 0; i < 4; ++i) v[i] = ld16_sc1(rs, gmo + i * 1024);
#pragma unroll
                for (int i = 0; i < 4; ++i) ok = ok & (v[i].x != SENT) & (v[i].y != SENT) & (v[i].z != SENT) & (v[i].w != SENT);
                if (__all(ok)) break;
                if (dead || ++spins > CH_CAP) { dead = true; if (lane == 0) st4_sc1(a.tmo, (unsigned)t * 8u + 1u); break; }
            }
            { RELANE(); CH_FCOND(); pre_ok = __all((!fc0 || f0 != 0u) && (!fc1 || f1 != 0u) && (!fc2 || f2 != 0u) && (!fc3 || f3 != 0u)); }
            LAS unsigned char* gl = ov + w * 4096 + (2 * (lane >> 5) + (lane & 1)) * 256 + ((lane >> 1) & 15) * 16;
#pragma unroll
            for (int i = 0; i < 4; ++i) *(LAS u32x4*)(gl + i * 1024) = v[i];
        }
        STAMP(1);
        LBAR();
        STAMP(2);
        f32x4 aqk = {0.f, 0.f, 0.f, 0.f};
        {
            LAS unsigned char* mf = ov + lane * 16;
            const int kofs = kh * 16384;
            bf16x8 bb[3];
            bb[0] = *(LAS bf16x8*)(mf + kofs); bb[1] = *(LAS bf16x8*)(mf + kofs + 1024);
#pragma unroll
            for (int i = 0; i < 32; ++i) {
                if (i + 2 < 32) bb[(i + 2) % 3] = *(LAS bf16x8*)(mf + (((i + 2) * 1024) & 16383) + (i + 2 < 16 ? kofs : (16384 - kofs)));
                __builtin_amdgcn_sched_barrier(0);
                aqk = __builtin_amdgcn_mfma_f32_16x16x32_bf16(wqk[i], bb[i % 3], aqk, 0, 0, 0);
                __builtin_amdgcn_sched_barrier(0);
            }
        }
        STAMP(3);
        u32x2 x2; x2.x = cvt_pk_bf16(aqk[0], aqk[1]); x2.y = cvt_pk_bf16(aqk[2], aqk[3]);
        *(LAS u32x2*)(lds + CH_WL + ((g4 >> 1) * 2 + (w >> 2)) * 4096 + (w & 3) * 256 + tok * 16 + (g4 & 1) * 8) = x2;
        STAMP(4);
        LBAR();
        STAMP(5);
        f32x4 av = {0.f, 0.f, 0.f, 0.f};
        const unsigned kvo = (unsigned)(((slot * 2) * KVR + (sseg & (KVR - 1))) * 32 + h) * 2048 + (tok & 7) * 256 + hf * 128 + a.o_kv;
        {
            LAS unsigned char* qf = lds + CH_WL + lane * 16;
            LAS unsigned char* mf = ov + kh * 16384 + lane * 16;
            bf16x8 bb[3];
            bb[0] = *(LAS bf16x8*)mf; bb[1] = *(LAS bf16x8*)(mf + 1024);
            const bf16x8 q0 = *(LAS bf16x8*)qf, q1 = *(LAS bf16x8*)(qf + 4096);
            if (w == 4) { kx[0] = *(LAS u32x4*)(qf + 8192); kx[1] = *(LAS u32x4*)(qf + 12288); }
            f32x4 sx = {0.f, 0.f, 0.f, 0.f};
#pragma unroll
            for (int i = 0; i < 16; ++i) {
                if (i + 2 < 16) bb[(i + 2) % 3] = *(LAS bf16x8*)(mf + (i + 2) * 1024);
                __builtin_amdgcn_sched_barrier(0);
                av = __builtin_amdgcn_mfma_f32_16x16x32_bf16(wv[i], bb[i % 3], av, 0, 0, 0);
                __builtin_amdgcn_sched_barrier(0);
                if (i == 0) { sx = __builtin_amdgcn_mfma_f32_16x16x32_bf16(__builtin_bit_cast(bf16x8, kx[0]), q0, sx, 0, 0, 0); __builtin_amdgcn_sched_barrier(0); }
                if (i == 1) { sx = __builtin_amdgcn_mfma_f32_16x16x32_bf16(__builtin_bit_cast(bf16x8, kx[1]), q1, sx, 0, 0, 0); __builtin_amdgcn_sched_barrier(0); }
                if (i == 2) {
                    st16_sc1(tag4(sx, tag), rs, ((w < 4 ? ((w >> 1) ? act1 : act0) : w == 4) ? (unsigned)(c * XSTR + w * 1024) + a.o_X : OOB) + (unsigned)lane * 16u);
                    __builtin_amdgcn_sched_barrier(0); }
                if (i == 4) {
                    if (act && g4 >= 2) st8b_sc1(x2, rs, kvo + 16 * w + 8 * (g4 - 2));
                    __builtin_amdgcn_sched_barrier(0); }
            }
        }
        STAMP(6);
        const u32x4 avt = tag4(av, tag);
        st16_sc1(avt, rs, act ? (unsigned)(c * XSTR + 8192 + w * 1024 + lane * 16) + a.o_X : OOB);
        if (w < 4) *(LAS u32x4*)(lds + CH_WL + (4 + w) * 4096 + lane * 16) = avt;
        LBAR();
        RELANE();
        LAS unsigned char* vxd = ov + kh * OV_V1 + (8 + (w & 3) * 8 + (lane >> 3)) * 272 + (lane & 7) * 32;
        if (w >= 4) {
            const unsigned pa = act ? (unsigned)((c ^ 1) * XSTR + 8192 + (w - 4) * 1024 + lane * 16) + a.o_X : OOB;
            unsigned spins = 0;
            const u32x4 a0 = *(LAS u32x4*)(lds + CH_WL + w * 4096 + lane * 16);
            u32x2 vb; vb.x = cvt_pk_bf16(untag(a0.x) + untag(avt.x), untag(a0.y) + untag(avt.y)); vb.y = cvt_pk_bf16(untag(a0.z) + untag(avt.z), untag(a0.w) + untag(avt.w));
            LAS unsigned char* vmd = ov + slot * OV_V1 + (tok & 7) * 272 + (16 * (w - 4) + 4 * g4) * 2;
            *(LAS u32x2*)(vmd + hf * 128) = vb;
            u32x4 p0 = ld16_sc1(rs, pa), p1 = ld16_sc1(rs, pa + 4096u);
            *(LAS u32x4*)vxd = vx0; *(LAS u32x4*)(vxd + 16) = vx1;
            if (act) st8b_sc1(vb, rs, kvo + KVR * 32 * 2048 + 32 * (w - 4) + 8 * g4);
            STAMP(7);
            for (;;) {
                if (__all((!act) | (((or4x(p0, tag) | or4x(p1, tag)) & 1u) == 0u))) break;
                if (dead || ++spins > CH_CAP) { dead = true; if (lane == 0) st4_sc1(a.tmo, (unsigned)t * 8u + 3u); break; }
                p0 = ld16_sc1(rs, pa); p1 = ld16_sc1(rs, pa + 4096u);
            }
            u32x2 o; o.x = cvt_pk_bf16(untag(p0.x) + untag(p1.x), untag(p0.y) + untag(p1.y)); o.y = cvt_pk_bf16(untag(p0.z) + untag(p1.z), untag(p0.w) + untag(p1.w));
            *(LAS u32x2*)(vmd + (hf ^ 1) * 128) = o;
        } else {
            const bool actq = (w & 2) ? act1 : act0; const unsigned tagq = (w & 2) ? tag1 : tag0;
            const int gr = tok < 10 ? tok : 9;
            const unsigned so_ = (actq ? (unsigned)(c * XSTR + w * 64) + a.o_X : OOB) + (unsigned)(g4 * 16 + (gr < 2 ? 4096 + ((w & 2) + gr) * 256 : ((w & 2) + ((gr - 2) >> 2)) * 1024 + ((gr - 2) & 3) * 256));
            *(LAS u32x4*)vxd = vx0; *(LAS u32x4*)(vxd + 16) = vx1;
            STAMP(7);
            if (X1) __builtin_amdgcn_s_sleep(X1);
            u32x4 ps, po; unsigned spins = 0;
            for (;;) {
                ps = ld16_sc1(rs, so_ ^ (unsigned)XSTR); po = ld16_sc1(rs, so_);
                if (__all((!actq) | (((or4x(ps, tagq) | or4x(po, tagq)) & 1u) == 0u))) break;
                if (dead || ++spins > CH_CAP) { dead = true; if (lane == 0) st4_sc1(a.tmo, (unsigned)t * 8u + 4u); break; }
            }
            float sv[4]; float m = NEGB;
#pragma unroll
            for (int j = 0; j < 4; ++j) { const float s = untag(po[j]) + untag(ps[j]); sv[j] = rbf(rbf(s) * 0.08838834764831845f); m = fmaxf(m, sv[j]); }
            if (tok >= 10) m = NEGB;
            m = row16_max(m);
            float e[4], d = 0.f;
#pragma unroll
            for (int j = 0; j < 4; ++j) { e[j] = tok < 10 ? __expf(sv[j] - m) : 0.f; d += e[j]; }
            d = row16_sum(d);
            const float inv = 1.0f / d;
            u32x2 pw; pw.x = cvt_pk_bf16(e[0] * inv, e[1] * inv); pw.y = cvt_pk_bf16(e[2] * inv, e[3] * inv);
            if (tok < 12) *(LAS u32x2*)(ov + OV_P + (4 * w + g4) * 96 + tok * 8) = pw;
        }
        STAMP(8);
        RELANE();
        LAS unsigned char* wl = lds + CH_WL + w * 4096 + lane * 16;
        bf16x8 wa[WPRE];
        wa[0] = wo0;
#pragma unroll
        for (int i = 1; i < WPRE; ++i) wa[i] = *(LAS bf16x8*)(wl + (i >> 2) * 32768 + (i & 3) * 1024);
        const int q4 = tok >> 2, p4 = lane & 3;
        LAS unsigned char* pr = ov + OV_P + tok * 96;
        LAS unsigned char* va = ov + (8 * g4 + q4) * 272 + (16 * w + 4 * p4) * 2;
        LAS unsigned char* vb2 = ov + (32 + 4 * g4 + q4 > 39 ? 39 : 32 + 4 * g4 + q4) * 272 + (16 * w + 4 * p4) * 2;
        LAS unsigned char* od = ov + OV_O + (w >> 1) * 1024 + (2 * (w & 1) + (g4 >> 1)) * 256 + tok * 16 + (g4 & 1) * 8;
        __builtin_amdgcn_sched_barrier(0);
        LBAR();
        STAMP(9);
        kx_ok = pre_ok;
        {
            const u32x4 p0 = *(LAS u32x4*)(pr + g4 * 16);
            const u32x2 p1 = *(LAS u32x2*)(pr + 64 + g4 * 8);
            f32x4 oa[2];
#pragma unroll
            for (int cc = 0; cc < 2; ++cc) {
                const v4s lo = __builtin_amdgcn_ds_read_tr16_b64_v4i16((LAS v4s*)(va + cc * OV_V1));
                const v4s hi = __builtin_amdgcn_ds_read_tr16_b64_v4i16((LAS v4s*)(va + cc * OV_V1 + 4 * 272));
                const v4s l2 = __builtin_amdgcn_ds_read_tr16_b64_v4i16((LAS v4s*)(vb2 + cc * OV_V1));
                bf16x8 vf; vf[0] = lo[0]; vf[1] = lo[1]; vf[2] = lo[2]; vf[3] = lo[3]; vf[4] = hi[0]; vf[5] = hi[1]; vf[6] = hi[2]; vf[7] = hi[3];
                oa[cc] = __builtin_amdgcn_mfma_f32_16x16x32_bf16(vf, __builtin_bit_cast(bf16x8, p0), (f32x4){0.f, 0.f, 0.f, 0.f}, 0, 0, 0);
                oa[cc] = __builtin_amdgcn_mfma_f32_16x16x16bf16_1k(l2, __builtin_bit_cast(bf16x4, p1), oa[cc], 0, 0, 0);
            }
            {
                const int t1 = t + 1, u1 = t1 - L;
                const unsigned n0 = (pre_ok && t1 < 64) ? (unsigned)t1 * 786432u + h * 768 + a.o_qkv : OOB, n1 = (pre_ok && u1 >= 0 && u1 < 64) ? (unsigned)u1 * 786432u + a.qstride + h * 768 + a.o_qkv : OOB;
                KX_LOAD(n0, n1); }
            u32x2 ob; ob.x = cvt_pk_bf16(slot ? oa[1][0] : oa[0][0], slot ? oa[1][1] : oa[0][1]); ob.y = cvt_pk_bf16(slot ? oa[1][2] : oa[0][2], slot ? oa[1][3] : oa[0][3]);
            *(LAS u32x2*)od = ob;
        }
        const unsigned uo = act ? (unsigned)(((hf * 32 + w) * 32 + h) * 1024 + tok * 64 + g4 * 16) + a.o_U : OOB;
        LAS unsigned char* of_ = ov + OV_O + lane * 16;
        wait_vm<2>();
        LBAR();
        STAMP(10);
        {
            bf16x8 of[4];
#pragma unroll
            for (int ks = 0; ks < 4; ++ks) of[ks] = *(LAS bf16x8*)(of_ + ks * 1024);
#pragma unroll
            for (int cg = 0; cg < 4; ++cg) {
                f32x4 u = {0.f, 0.f, 0.f, 0.f};
#pragma unroll
                for (int ks = 0; ks < 4; ++ks) { const int f = cg * 4 + ks;
                    u = __builtin_amdgcn_mfma_f32_16x16x32_bf16(wa[f % WPRE], of[ks], u, 0, 0, 0);
                    __builtin_amdgcn_sched_barrier(0);
                    if (f + WPRE < 16) wa[f % WPRE] = *(LAS bf16x8*)(wl + ((f + WPRE) >> 2) * 32768 + ((f + WPRE) & 3) * 1024);
                    __builtin_amdgcn_sched_barrier(0); }
                st16_sc1(tag4(u, tag), rs, uo + cg * (8 * 32 * 1024));
                __builtin_amdgcn_sched_barrier(0);
            }
        }
        if (w == 1) {
            if (act0 && (s0 % SPB) == SPB - 1 && lane == 0) __builtin_amdgcn_raw_buffer_store_b32((unsigned)t + 1u, rs, 0, o_ctr + (unsigned)(FLG(0, PH_ARDY, s0 / SPB) + c) * (4u * FSTR), 16);
            if (act1 && (s1 % SPB) == SPB - 1 && lane == 0) __builtin_amdgcn_raw_buffer_store_b32((unsigned)t + 1u, rs, 0, o_ctr + (unsigned)(FLG(1, PH_ARDY, s1 / SPB) + c) * (4u * FSTR), 16);
        }
        STAMP(11);
        RELANE();
        {
            const bool actw = w < 4 ? act0 : act1; const unsigned tagw = w < 4 ? tag0 : tag1;
            const unsigned uoff = (actw ? (unsigned)(c * 32768 + w * 128) + a.o_U : OOB) + (unsigned)((lane >> 3) * 1024 + (lane & 7) * 16);
            const int col = 4 * (lane & 3) + 2 * ((lane >> 4) & 1), tk = 2 * w + ((lane >> 2) & 1);
            const bool ini = t + 1 == L && w >= 4;
            const unsigned mo = ((actw || ini) && !(lane & 40)) ? (unsigned)((t + 1) * 32768 + c * 512 + tk * 32 + col * 2) + a.o_mem : OOB;
            u32x4 uu[4]; unsigned spins = 0;
            if (U0) __builtin_amdgcn_s_sleep(U0);
            for (;;) {
#pragma unroll
                for (int i = 0; i < 4; ++i) uu[i] = ld16_sc1(rs, uoff + i * 8192);
                if ((!actw) | __all(((or4x(uu[0], tagw) | or4x(uu[1], tagw) | or4x(uu[2], tagw) | or4x(uu[3], tagw)) & 1u) == 0u)) break;
                if (dead || ++spins > CH_CAP) { dead = true; if (lane == 0) st4_sc1(a.tmo, (unsigned)t * 8u + 2u); break; }
            }
            STAMP(12);
            float r[4];
#pragma unroll
            for (int j = 0; j < 4; ++j) r[j] = (untag(uu[0][j]) + untag(uu[1][j])) + (untag(uu[2][j]) + untag(uu[3][j]));
            const auto a01 = __builtin_amdgcn_permlane32_swap(__float_as_uint(r[0]), __float_as_uint(r[1]), false, false), a23 = __builtin_amdgcn_permlane32_swap(__float_as_uint(r[2]), __float_as_uint(r[3]), false, false);
            const float s01 = __uint_as_float(a01[0]) + __uint_as_float(a01[1]), s23 = __uint_as_float(a23[0]) + __uint_as_float(a23[1]);
            const auto b = __builtin_amdgcn_permlane16_swap(__float_as_uint(s01), __float_as_uint(s23), false, false);
            float sr = __uint_as_float(b[0]) + __uint_as_float(b[1]);
            sr += dpp_f<0x128>(sr);
            const auto f = __builtin_amdgcn_permlane32_swap(__float_as_uint(sr), __float_as_uint(sr), false, false);
            unsigned o = cvt_pk_bf16(__uint_as_float(f[0]), __uint_as_float(f[1]));
            if (ini) o = *(const unsigned*)(a.mem0 + (tk & 7) * 1024 + c * 16 + col);
            if (o == SENT) o = QNAN;
            __builtin_amdgcn_raw_buffer_store_b32(o, rs, mo, 0, 16);
            STAMP(13);
        }
    }
}
#define WSUM_DPP 1
__device__ __forceinline__ float wave_sum(float v) {
    v = row16_sum(v);
    { const auto s = __builtin_amdgcn_permlane16_swap(__float_as_uint(v), __float_as_uint(v), false, false); v = __uint_as_float(s[0]) + __uint_as_float(s[1]); }
    { const auto s = __builtin_amdgcn_permlane32_swap(__float_as_uint(v), __float_as_uint(v), false, false); v = __uint_as_float(s[0]) + __uint_as_float(s[1]); }
    return v;
}
__device__ __forceinline__ void ln16_core(const float (&x)[16], float (&y)[16]) {
    float s = 0.f;
#pragma unroll
    for (int j = 0; j < 16; ++j) s += x[j];
    const float mean = wave_sum(s) * (1.0f / 1024.0f);
    float q = 0.f;
#pragma unroll
    for (int j = 0; j < 16; ++j) { const float d = x[j] - mean; q += d * d; }
    const float var = rbf(wave_sum(q) * (1.0f / 1024.0f));
    const float mb = rbf(mean);
    const float sd = rbf(sqrtf(rbf(var + 1e-5f)));
    const float inv = 1.0f / sd;
#pragma unroll
    for (int j = 0; j < 16; ++j) y[j] = rbf(rbf(x[j] - mb) * inv);
}
__device__ __forceinline__ void ln16(const float (&x)[16], __amdgpu_buffer_rsrc_t rs, unsigned dsto, int lane, bool twice) {
    float y[16];
    ln16_core(x, y);
    if (twice) { float z[16]; ln16_core(y, z);
#pragma unroll
        for (int j = 0; j < 16; ++j) y[j] = z[j]; }
    u32x4 a, b;
    a.x = cvt_pk_bf16(y[0], y[1]); a.y = cvt_pk_bf16(y[2], y[3]); a.z = cvt_pk_bf16(y[4], y[5]); a.w = cvt_pk_bf16(y[6], y[7]);
    b.x = cvt_pk_bf16(y[8], y[9]); b.y = cvt_pk_bf16(y[10], y[11]); b.z = cvt_pk_bf16(y[12], y[13]); b.w = cvt_pk_bf16(y[14], y[15]);
    st16_sc1(a, rs, dsto + lane * 16); st16_sc1(b, rs, dsto + 1024 + lane * 16);
}
__device__ __forceinline__ void unpack8(u32x4 v, float* x) { x[0] = bflo(v.x); x[1] = bfhi(v.x); x[2] = bflo(v.y); x[3] = bfhi(v.y); x[4] = bflo(v.z); x[5] = bfhi(v.z); x[6] = bflo(v.w); x[7] = bfhi(v.w); }

__device__ __forceinline__ void wait_vm_n(int n) {
    switch (n) { case 0: wait_vm<0>(); break; case 1: wait_vm<1>(); break; case 2: wait_vm<2>(); break; case 3: wait_vm<3>(); break; case 4: wait_vm<4>(); break; case 5: wait_vm<5>(); break; case 6: wait_vm<6>(); break; case 7: wait_vm<7>(); break;
        case 8: wait_vm<8>(); break; case 9: wait_vm<9>(); break; case 10: wait_vm<10>(); break; case 11: wait_vm<11>(); break; case 12: wait_vm<12>(); break; case 13: wait_vm<13>(); break; case 14: wait_vm<14>(); break;
        case 15: wait_vm<15>(); break; case 16: wait_vm<16>(); break; default: wait_vm<0>(); break; }
}
#define WS_SB() __builtin_amdgcn_sched_barrier(0)
#define WS_BAR() do { __builtin_amdgcn_s_barrier(); asm volatile("" ::: "memory"); } while (0)

__device__ __forceinline__ void ws_load_weights(LAS unsigned char* lds, const bf16_t* W, int ldb, unsigned half2, int nst, bf16x8 (&bw)[16][2]) {
    int tid = threadIdx.x; asm volatile("" : "+v"(tid));
    const int wid = __builtin_amdgcn_readfirstlane(tid >> 6), lane = tid & 63, kg = wid >> 2, wn = wid & 3, fr = lane & 15, fq = lane >> 4, q = fr >> 2, p = fr & 3;
    const int r0 = wid * 4 + (lane >> 4), sg = (lane & 15) >> 1, hf = lane & 1, sw = (r0 & 3) | (((r0 >> 3) & 1) << 2);
    const char* src = (const char*)W + ((size_t)r0 * ldb + ((sg ^ sw) << 4) + hf * 8) * 2;
    const int bsw = q | ((fq & 1) << 2);
#define WSL 0
#pragma unroll
    for (int rd = 0; rd < 2; ++rd) {
#pragma unroll
        for (int st = 0; st < 4; ++st) {
            if (rd * 4 + st == 7 && nst < 8) continue;
#pragma unroll
            for (int i = 0; i < 4; ++i)
                __builtin_amdgcn_global_load_lds((const unsigned*)(src + ((size_t)rd * half2 + (size_t)(st * 128 + i * 32) * ldb) * 2), (LAS unsigned*)(lds + st * 32768 + (i * 8 + wid) * 1024), 16, 0, 0);
        }
        asm volatile("s_waitcnt vmcnt(0)" ::: "memory");
        __syncthreads();
#pragma unroll
        for (int st = 0; st < 4; ++st)
#pragma unroll
            for (int j = 0; j < 2; ++j)
#pragma unroll
                for (int n = 0; n < 2; ++n) {
                    LAS unsigned char* a0 = lds + st * 32768 + ((2 * kg + j) * 32 + 8 * fq + q) * 256 + (((2 * wn + n) ^ bsw) << 5) + p * 8;
                    bw[(rd * 4 + st) * 2 + j][n] = tr_frag_at(a0);
                }
        asm volatile("s_waitcnt lgkmcnt(0)" ::: "memory");
        __syncthreads();
    }
}

#define PSTG 5
#define NAF 6
#define WS_ASC ""
#define NUP 2
#define WS_DMA(s_, i_) do { const unsigned long long _g = abase + (unsigned)((s_) * 256) + (unsigned long long)(i_) * rstep; const unsigned _l = lbase + (unsigned)((s_) * (RG * 4096) + (i_) * 8192) + (unsigned)wid * 1024u; \
        asm volatile("s_mov_b32 m0, %0\n\tglobal_load_lds_dwordx4 %1, %2" WS_ASC :: "s"(_l), "v"(voffA), "s"(_g) : "memory", "m0"); } while (0)
#define WS_DMA_SETUP() \
    const unsigned sb = (unsigned)lane * 16u, swz = sb ^ (((sb >> 9) & 1u) << 5); \
    const unsigned voffA = (unsigned)(((wid >> 2) * 16 + (int)(swz >> 6)) * lda + (wid & 3) * 32 + (int)((swz & 63u) >> 1)) * 2u; \
    const unsigned lbase = (unsigned)(size_t)lds; \
    const unsigned long long abase = (unsigned long long)A; \
    const unsigned rstep = (unsigned)lda * 64u
__device__ __forceinline__ void ws_issue(LAS unsigned char* lds, const bf16_t* A, int lda, int ns) {
    int tid = threadIdx.x; asm volatile("" : "+v"(tid));
    const int wid = __builtin_amdgcn_readfirstlane(tid >> 6), lane = tid & 63;
    WS_DMA_SETUP();
#pragma unroll
    for (int s = 0; s < NUP; ++s)
        if (s < 7 || ns == 8) {
#pragma unroll
            for (int i = 0; i < RG / 2; ++i) WS_DMA(s, i);
        }
}
#define TWIN 0
#define PCT 1400
#define POVER 300
#define DGEMM 520
#define DATT 680
#define DRED 500
__device__ __forceinline__ unsigned ws_now() { return (unsigned)__builtin_amdgcn_s_memrealtime(); }
__device__ __forceinline__ unsigned ws_time_mask(unsigned tdue) {
    if (!TWIN || tdue == 0u) return 128u | 256u | 512u;
    const int rem = (int)(tdue - ws_now());
    if (rem < -POVER) return 128u | 256u | 512u;
    return (rem >= DATT ? 128u : 0u) | (rem >= DRED ? 256u : 0u) | (rem >= DGEMM ? 512u : 0u);
}
struct PollOff { unsigned o0, o1, o2, o3; };
__device__ __forceinline__ unsigned ws_poll_ld(const unsigned* ctr, unsigned o) { unsigned v = 1u; if (o != 0xFFFFFFFFu) v = __hip_atomic_load((GAS unsigned*)(ctr + o * FSTR), __ATOMIC_RELAXED, __HIP_MEMORY_SCOPE_AGENT); return v; }
__device__ __forceinline__ unsigned ws_poll_mask(unsigned v0, unsigned v1, unsigned v2, unsigned v3) {
    const unsigned long long z0 = __ballot(v0 == 0u), z1 = __ballot(v1 == 0u), z2 = __ballot(v2 == 0u), z3 = __ballot(v3 == 0u);
    return (z0 == 0ull ? 1u : 0u) | ((unsigned)z1 == 0u ? 2u : 0u) | ((unsigned)(z1 >> 32) == 0u ? 4u : 0u) | ((unsigned)z2 == 0u ? 8u : 0u) | ((unsigned)(z2 >> 32) == 0u ? 16u : 0u) |
           ((unsigned)z3 == 0u ? 32u : 0u) | ((unsigned)(z3 >> 32) == 0u ? 64u : 0u);
}
#define ABRT 0
template <int NS> constexpr int ws_pd() { return NUP >= NS ? 3 : NUP; }
template <int NS> constexpr int ws_younger(int k) {
    constexpr int PPW = RG / 2, PD = ws_pd<NS>(), NP = ABRT ? NS - 1 - PD : 0;
    if (NUP >= NS) {
        const int np = k < NP ? k : NP;
        if (ABRT && k >= PD && k < NS - 1) return np - (k - PD + 1);
        return (NS - 1 - k) * PPW + np;
    }
    int n = k < NUP ? (NUP - 1 - k) * PPW : 0;
    for (int s = (k < NUP ? 0 : k - NUP + 1); s <= k - 1 && s + NUP < NS; ++s) n += PPW + (s < NP ? 1 : 0);
    return n;
}
template <int NS, class PF> __device__ __forceinline__ unsigned ws_compute(LAS unsigned char* lds, const bf16_t* A, int lda, const bf16x8 (&bw)[16][2], f32x4 (&res)[1][RG / 2][2], const unsigned* ctr, PF&& pollf, unsigned tdue,
                                                                 unsigned ab_base, unsigned ab_last, bool can_abort) {
    constexpr int NT = 1;
    int tid = threadIdx.x; asm volatile("" : "+v"(tid));
    const int wid = __builtin_amdgcn_readfirstlane(tid >> 6), lane = tid & 63, kg = wid >> 2, fr = lane & 15, fq = lane >> 4;
    constexpr int PPW = RG / 2, STB = RG * 4096, SPS = 2 * RG, HB = 65536 / STB;
    WS_DMA_SETUP();
    const unsigned ob = (unsigned)(fr * 64 + fq * 16);
    const unsigned fa = lbase + (unsigned)(2 * kg) * 1024u + (ob ^ (((ob >> 9) & 1u) << 5));
    unsigned fb[4] = {fa + kg * (PPW * 4096u), fa + kg * (PPW * 4096u) + 65536u, fa + (kg ^ 1) * (PPW * 4096u), fa + (kg ^ 1) * (PPW * 4096u) + 65536u};
    asm volatile("" : "+v"(fb[0]), "+v"(fb[1]), "+v"(fb[2]), "+v"(fb[3]));
#define WS_RD(g_) (*(const LAS bf16x8*)(size_t)(fb[((((g_) % RG) / PPW) & 1) * 2 + ((g_) / SPS) / HB] + (unsigned)((((g_) / SPS) % HB) * STB + ((g_) % PPW) * 4096 + (((g_) / RG) & 1) * 1024)))
    f32x4 acc[NT][RG][2];
#pragma unroll
    for (int t = 0; t < NT; ++t)
#pragma unroll
        for (int m = 0; m < RG; ++m) { acc[t][m][0] = (f32x4){0.f, 0.f, 0.f, 0.f}; acc[t][m][1] = (f32x4){0.f, 0.f, 0.f, 0.f}; }
    unsigned v0 = 1u, v1 = 1u, v2 = 1u, v3 = 1u;
#define WS_LANDED(s_) do { wait_vm_n(ws_younger<NS>(s_)); WS_BAR(); } while (0)
    constexpr int PD = ws_pd<NS>();
    const unsigned pslot = lbase + (unsigned)(NS * STB);
    unsigned abv = 0u;
    WS_LANDED(0);
    bf16x8 af[NAF];
#pragma unroll
    for (int g = 0; g < NAF - 1; ++g) af[g] = WS_RD(g);
#pragma unroll
    for (int s = 0; s < NS; ++s) {
#pragma unroll
        for (int f = 0; f < SPS; ++f) {
            const int g = s * SPS + f, gn = g + NAF - 1;
            WS_SB();
            if (ABRT && f == 0 && s < NS - 1 - PD) {
                unsigned l2 = (unsigned)lane; asm volatile("" : "+v"(l2));
                const unsigned oab = (ab_base + (l2 < ab_last ? l2 : ab_last)) * (4u * FSTR);
                if ((l2 >> 3) == (unsigned)wid) asm volatile("s_mov_b32 m0, %0\n\tglobal_load_lds_dword %1, %2 sc1" :: "s"(pslot + (unsigned)(s * 256)), "v"(oab), "s"(ctr) : "memory", "m0");
            }
            if (f < PPW && s + NUP < NS) WS_DMA(s + NUP, f);
            WS_SB();
#pragma unroll
            for (int t = 0; t < NT; ++t) {
                acc[t][g % RG][0] = __builtin_amdgcn_mfma_f32_16x16x32_bf16(bw[t * 2 * NS + 2 * s + f / RG][0], af[g % NAF], acc[t][g % RG][0], 0, 0, 0);
                acc[t][g % RG][1] = __builtin_amdgcn_mfma_f32_16x16x32_bf16(bw[t * 2 * NS + 2 * s + f / RG][1], af[g % NAF], acc[t][g % RG][1], 0, 0, 0);
            }
            WS_SB();
            if (gn < NS * SPS) {
                if (gn % SPS == 0) { WS_LANDED(gn / SPS);
                    if (ABRT && gn / SPS >= PD && gn / SPS < NS - 1 && can_abort) { unsigned l2 = (unsigned)lane; asm volatile("" : "+v"(l2)); abv = *(const LAS unsigned*)(size_t)(pslot + (unsigned)((gn / SPS - PD) * 256) + l2 * 4u); } }
                af[gn % NAF] = WS_RD(gn);
            }
            if (ABRT && f == SPS - 2 && s + 1 >= PD && s + 1 < NS - 1 && can_abort) { if (__all(abv != 0u)) return 0x80000000u; }
            if (s == NS - 3 && f == 1 && wid == 0) {
                int l2 = lane; asm volatile("" : "+v"(l2));
                PollOff po; pollf(l2, po);
                v0 = ws_poll_ld(ctr, po.o0); v1 = ws_poll_ld(ctr, po.o1); v2 = ws_poll_ld(ctr, po.o2); v3 = ws_poll_ld(ctr, po.o3);
            }
        }
    }
    WS_SB();
    unsigned ok;
    {
        LAS unsigned char* wp = lds + NS * STB + wid * (NT * PPW * 2048) + lane * 16; LAS unsigned char* rp = lds + NS * STB + (wid ^ 4) * (NT * PPW * 2048) + lane * 16;
#pragma unroll
        for (int t = 0; t < NT; ++t)
#pragma unroll
            for (int mi = 0; mi < PPW; ++mi)
#pragma unroll
                for (int n = 0; n < 2; ++n) *(LAS f32x4*)(wp + ((t * PPW + mi) * 2 + n) * 1024) = acc[t][PPW + mi][n];
        if (wid == 0) {
            const unsigned st = ws_poll_mask(v0, v1, v2, v3) | ws_time_mask(tdue);
            if (lane < 8) *(LAS unsigned*)(lds + lane * 1024) = st;
        }
        asm volatile("s_waitcnt lgkmcnt(0)" ::: "memory");
        WS_BAR();
        ok = *(LAS unsigned*)(lds + wid * 1024);
#pragma unroll
        for (int t = 0; t < NT; ++t)
#pragma unroll
            for (int mi = 0; mi < PPW; ++mi)
#pragma unroll
                for (int n = 0; n < 2; ++n) { const f32x4 o = *(LAS f32x4*)(rp + ((t * PPW + mi) * 2 + n) * 1024); res[t][mi][n] = acc[t][mi][n] + o; }
    }
#undef WS_RD
#undef WS_LANDED
    return __builtin_amdgcn_readfirstlane(ok);
}
__device__ __forceinline__ float gelu_tanh(float x) {
    const float u = 0.7978845608028654f * (x + 0.044715f * x * x * x);
    const float e = __expf(-2.0f * u);
    return x * __builtin_amdgcn_rcpf(1.0f + e);
}
__device__ __forceinline__ void ws_store(const f32x4 (&res)[RG / 2][2], int mode, __amdgpu_buffer_rsrc_t rs, unsigned c_off, int ldc, const bf16_t* bias) {
    int tid = threadIdx.x; asm volatile("" : "+v"(tid));
    const int wid = __builtin_amdgcn_readfirstlane(tid >> 6), lane = tid & 63, kg = wid >> 2, wn = wid & 3, fr = lane & 15, fq = lane >> 4;
    const int row0 = kg * (RG * 8) + fr;
    if (mode == 2) {
        const unsigned o = c_off + ((unsigned)row0 * ldc + wn * 32 + 4 * fq) * 4u;
#pragma unroll
        for (int mi = 0; mi < RG / 2; ++mi)
#pragma unroll
            for (int n = 0; n < 2; ++n) st16_sc1(__builtin_bit_cast(u32x4, res[mi][n]), rs, o + (unsigned)(mi * 16 * ldc) * 4u + n * 64);
    } else {
        float bv0[4] = {0.f, 0.f, 0.f, 0.f}, bv1[4] = {0.f, 0.f, 0.f, 0.f};
        if (mode == 1) { const u32x2 b0 = *(const u32x2*)(bias + wn * 32 + 4 * fq), b1 = *(const u32x2*)(bias + wn * 32 + 16 + 4 * fq);
            bv0[0] = bflo(b0.x); bv0[1] = bfhi(b0.x); bv0[2] = bflo(b0.y); bv0[3] = bfhi(b0.y); bv1[0] = bflo(b1.x); bv1[1] = bfhi(b1.x); bv1[2] = bflo(b1.y); bv1[3] = bfhi(b1.y); }
        const unsigned o = c_off + ((unsigned)row0 * ldc + wn * 32 + (fq & 1) * 16 + (fq >> 1) * 8) * 2u;
#pragma unroll
        for (int mi = 0; mi < RG / 2; ++mi) {
            f32x4 v0 = res[mi][0], v1 = res[mi][1];
            if (mode == 1) {
#pragma unroll
                for (int j = 0; j < 4; ++j) { v0[j] = gelu_tanh(rbf(rbf(v0[j]) + bv0[j])); v1[j] = gelu_tanh(rbf(rbf(v1[j]) + bv1[j])); }
            }
            const unsigned a0 = cvt_pk_bf16(v0[0], v0[1]), a1 = cvt_pk_bf16(v0[2], v0[3]), c0 = cvt_pk_bf16(v1[0], v1[1]), c1 = cvt_pk_bf16(v1[2], v1[3]);
            const auto s0 = __builtin_amdgcn_permlane16_swap(a0, c0, false, false), s1 = __builtin_amdgcn_permlane16_swap(a1, c1, false, false);
            u32x4 w; w.x = s0[0]; w.y = s1[0]; w.z = s0[1]; w.w = s1[1];
            st16_sc1(w, rs, o + (unsigned)(mi * 16 * ldc) * 2u);
        }
    }
}

template <int NR> __device__ __forceinline__ void red_task(unsigned slab_off, const bf16_t* bias, const bf16_t* xi_plain, unsigned xi_off, bf16_t* xo_plain, __amdgpu_buffer_rsrc_t rs, unsigned xo_off, unsigned xn_off, int row0) {
    int tid = threadIdx.x; asm volatile("" : "+v"(tid));
    const int lane = tid & 63;
    u32x4 sv[NR][4][4], xq[NR][2];
#pragma unroll
    for (int r = 0; r < NR; ++r) {
        const int row = row0 + r;
#pragma unroll
        for (int sidx = 0; sidx < 4; ++sidx) {
            const unsigned o = slab_off + ((unsigned)sidx * (S_ * DM) + (unsigned)row * DM + lane * 8) * 4u;
            sv[r][sidx][0] = __builtin_amdgcn_raw_buffer_load_b128(rs, o, 0, 16); sv[r][sidx][1] = __builtin_amdgcn_raw_buffer_load_b128(rs, o + 16, 0, 16);
            sv[r][sidx][2] = __builtin_amdgcn_raw_buffer_load_b128(rs, o + 2048, 0, 16); sv[r][sidx][3] = __builtin_amdgcn_raw_buffer_load_b128(rs, o + 2064, 0, 16);
        }
        if (xi_plain) { xq[r][0] = *(const u32x4*)(xi_plain + (size_t)row * DM + lane * 8); xq[r][1] = *(const u32x4*)(xi_plain + (size_t)row * DM + 512 + lane * 8); }
        else { const unsigned o = xi_off + (unsigned)row * 2048u + lane * 16; xq[r][0] = __builtin_amdgcn_raw_buffer_load_b128(rs, o, 0, 16); xq[r][1] = __builtin_amdgcn_raw_buffer_load_b128(rs, o + 1024, 0, 16); }
    }
    u32x4 bq[2] = {{0u, 0u, 0u, 0u}, {0u, 0u, 0u, 0u}};
    if (bias) { bq[0] = *(const u32x4*)(bias + lane * 8); bq[1] = *(const u32x4*)(bias + 512 + lane * 8); }
    __builtin_amdgcn_sched_barrier(0);
#pragma unroll
    for (int r = 0; r < NR; ++r) {
        const int row = row0 + r;
        float xv[16];
        unpack8(xq[r][0], xv); unpack8(xq[r][1], xv + 8);
#pragma unroll
        for (int q = 0; q < 4; ++q)
#pragma unroll
            for (int j = 0; j < 4; ++j) {
                float t = (__uint_as_float(sv[r][0][q][j]) + __uint_as_float(sv[r][1][q][j])) + (__uint_as_float(sv[r][2][q][j]) + __uint_as_float(sv[r][3][q][j]));
                t = rbf(t);
                if (bias) { const unsigned bwd = bq[q >> 1][(q & 1) * 2 + (j >> 1)]; t = rbf(t + ((j & 1) ? bfhi(bwd) : bflo(bwd))); }
                xv[q * 4 + j] = rbf(xv[q * 4 + j] + t);
            }
        u32x4 a, b;
        a.x = cvt_pk_bf16(xv[0], xv[1]); a.y = cvt_pk_bf16(xv[2], xv[3]); a.z = cvt_pk_bf16(xv[4], xv[5]); a.w = cvt_pk_bf16(xv[6], xv[7]);
        b.x = cvt_pk_bf16(xv[8], xv[9]); b.y = cvt_pk_bf16(xv[10], xv[11]); b.z = cvt_pk_bf16(xv[12], xv[13]); b.w = cvt_pk_bf16(xv[14], xv[15]);
        if (xo_plain) { *(u32x4*)(xo_plain + (size_t)row * DM + lane * 8) = a; *(u32x4*)(xo_plain + (size_t)row * DM + 512 + lane * 8) = b; }
        else { st16_sc1(a, rs, xo_off + (unsigned)row * 2048u + lane * 16); st16_sc1(b, rs, xo_off + (unsigned)row * 2048u + 1024 + lane * 16); ln16(xv, rs, xn_off + (unsigned)row * 2048u, lane, bias == nullptr); }
    }
}

__device__ __forceinline__ void attn_task(LAS unsigned char* lds, int seg, int h, int qt, unsigned qkv_off, unsigned km_off, unsigned vm_off, __amdgpu_buffer_rsrc_t rs, unsigned ob_off, const char* wsb) {
    int tid = threadIdx.x; asm volatile("" : "+v"(tid));
    const int w = __builtin_amdgcn_readfirstlane(tid >> 6), lane = tid & 63;
    const int fr = lane & 15, g4 = lane >> 4;
    LAS unsigned char* vl = lds + w * 64 * 272;
    const unsigned qb = qkv_off + ((unsigned)(seg * 32) * 12288u + h * 384) * 2u;
    const unsigned km = km_off + (unsigned)((seg & (KVR - 1)) * 32 + h) * 2048u;
    const unsigned vm = vm_off + (unsigned)((seg & (KVR - 1)) * 32 + h) * 2048u;
    bf16x8 qf[4], kf[4][3];
#pragma unroll
    for (int ks = 0; ks < 4; ++ks) qf[ks] = __builtin_bit_cast(bf16x8, __builtin_amdgcn_raw_buffer_load_b128(rs, qb + (unsigned)(qt * 16 + fr) * 24576u + ks * 64 + g4 * 16, 0, 16));
#pragma unroll
    for (int kt = 0; kt < 3; ++kt) { int key = kt * 16 + fr; if (key > 39) key = 39; const unsigned kr = key < 8 ? km + key * 256 : qb + (unsigned)(key - 8) * 24576u + 256;
#pragma unroll
        for (int ks = 0; ks < 4; ++ks) kf[ks][kt] = __builtin_bit_cast(bf16x8, __builtin_amdgcn_raw_buffer_load_b128(rs, kr + ks * 64 + g4 * 16, 0, 16)); }
    {
        const unsigned lb = (unsigned)(size_t)vl;
#pragma unroll
        for (int i = 0; i < 10; ++i) { const int key = i * 4 + g4;
            const unsigned p = (key < 8 ? vm + key * 256 : qb + (unsigned)(key - 8) * 24576u + 512) + (unsigned)((fr ^ ((key & 7) << 1)) * 16);
            asm volatile("s_mov_b32 m0, %0\n\tglobal_load_lds_dwordx4 %1, %2 sc1" :: "s"(lb + i * 1024u), "v"(p), "s"(wsb) : "memory", "m0"); }
    }
    __builtin_amdgcn_sched_barrier(0);
    f32x4 sc[3];
#pragma unroll
    for (int kt = 0; kt < 3; ++kt) sc[kt] = (f32x4){0.f, 0.f, 0.f, 0.f};
#pragma unroll
    for (int ks = 0; ks < 4; ++ks)
#pragma unroll
        for (int kt = 0; kt < 3; ++kt) sc[kt] = __builtin_amdgcn_mfma_f32_16x16x32_bf16(kf[ks][kt], qf[ks], sc[kt], 0, 0, 0);
    __builtin_amdgcn_sched_barrier(0);
#pragma unroll
    for (int i = 10; i < 16; ++i) *(LAS u32x4*)(vl + i * 1024 + lane * 16) = (u32x4){0u, 0u, 0u, 0u};
    unsigned pk[3][2];
    {
        const int qi = qt * 16 + fr; float sv[3][4]; float m = -3.0e38f;
#pragma unroll
        for (int kt = 0; kt < 3; ++kt)
#pragma unroll
            for (int j = 0; j < 4; ++j) { const int key = kt * 16 + 4 * g4 + j; float v = rbf(rbf(sc[kt][j]) * 0.08838834764831845f);
                const bool ok = key < 8 || (key < 40 && key - 8 <= qi); v = ok ? v : -1.0e9f; sv[kt][j] = v; m = fmaxf(m, v); }
        m = fmaxf(m, __shfl_xor(m, 16)); m = fmaxf(m, __shfl_xor(m, 32));
        float d = 0.f;
#pragma unroll
        for (int kt = 0; kt < 3; ++kt)
#pragma unroll
            for (int j = 0; j < 4; ++j) { const int key = kt * 16 + 4 * g4 + j; float e = key < 40 ? __expf(sv[kt][j] - m) : 0.f; sv[kt][j] = e; d += e; }
        d += __shfl_xor(d, 16); d += __shfl_xor(d, 32);
        const float inv = 1.0f / d;
#pragma unroll
        for (int kt = 0; kt < 3; ++kt) { pk[kt][0] = cvt_pk_bf16(sv[kt][0] * inv, sv[kt][1] * inv); pk[kt][1] = cvt_pk_bf16(sv[kt][2] * inv, sv[kt][3] * inv); }
    }
    asm volatile("s_waitcnt vmcnt(0) lgkmcnt(0)" ::: "memory");
    const int q4 = (lane & 15) >> 2, p4 = lane & 3;
    u32x2 ov[8];
#pragma unroll
    for (int et = 0; et < 8; ++et) {
        f32x4 oa = {0.f, 0.f, 0.f, 0.f};
#pragma unroll
        for (int kst = 0; kst < 2; ++kst) {
            LAS unsigned char* a0 = vl + ((2 * kst) * 16 + 4 * g4 + q4) * 256 + ((et ^ ((4 * g4 + q4) & 7)) << 5) + p4 * 8;
            v4s lo = __builtin_amdgcn_ds_read_tr16_b64_v4i16((LAS v4s*)a0);
            v4s hi = __builtin_amdgcn_ds_read_tr16_b64_v4i16((LAS v4s*)(a0 + 16 * 256));
            bf16x8 vf; vf[0] = lo[0]; vf[1] = lo[1]; vf[2] = lo[2]; vf[3] = lo[3]; vf[4] = hi[0]; vf[5] = hi[1]; vf[6] = hi[2]; vf[7] = hi[3];
            u32x4 pw; pw.x = pk[2 * kst][0]; pw.y = pk[2 * kst][1];
            if (kst == 0) { pw.z = pk[1][0]; pw.w = pk[1][1]; } else { pw.z = 0u; pw.w = 0u; }
            oa = __builtin_amdgcn_mfma_f32_16x16x32_bf16(vf, __builtin_bit_cast(bf16x8, pw), oa, 0, 0, 0);
        }
        ov[et].x = cvt_pk_bf16(oa[0], oa[1]); ov[et].y = cvt_pk_bf16(oa[2], oa[3]);
    }
    asm volatile("s_waitcnt lgkmcnt(0)" ::: "memory");
#pragma unroll
    for (int et = 0; et < 8; ++et) *(LAS u32x2*)(vl + fr * 272 + (16 * et + 4 * g4) * 2) = ov[et];
    asm volatile("s_waitcnt lgkmcnt(0)" ::: "memory");
#pragma unroll
    for (int i = 0; i < 4; ++i) { const int r = i * 4 + g4; const u32x4 o = *(LAS u32x4*)(vl + r * 272 + fr * 16);
        st16_sc1(o, rs, ob_off + (unsigned)((seg * 32 + qt * 16 + r) * 4096 + h * 128) * 2u + fr * 16); }
}
struct MegaArgs {
    ChainArgs ch;
    char* ws;
    const bf16_t* x; const bf16_t* b1; const bf16_t* b2; bf16_t* out; const bf16_t* w1; const bf16_t* w2;
    unsigned o_xn1, o_xa0, o_xna0, o_xb0, o_xnb0, o_xna1, o_qkv, o_ob, o_hb, o_slab, o_kv, o_xa1, o_fill, n_fill;
};
#define WSTAMP(i) do {} while (0)
typedef const __attribute__((address_space(4))) MegaArgs* KArgP;
#define MB (32 * SPB)
#define QSTRIDE (S_ * 12288u * 2u)
enum { R_KV = 0, R_Q = 1, R_OUT = 2, R_FFN1 = 3, R_FFN2 = 4, R_RED = 5 };
#define MERGE_LN 1
#define MERGE_FILL 1
#define WDQ 8
#define WDO 14
#define XHOLD 0
#define TBL 0
#define RED2 0
#define KVHOLD 0
#define ATHOLD 1
#define PSLEEP 2
#define QHOLD 3
#define HELP 1
enum { K_GEMM = 0, K_ATT = 1, K_RED = 2 };
struct Heads { int h0, h1, h2, h4; };
__device__ __forceinline__ void ws_poll_offsets(int role, int ti, const Heads& h, int lane, PollOff& po) {
    const int hi = lane >> 5, ix = lane & 31;
    po.o0 = po.o1 = po.o2 = po.o3 = 0xFFFFFFFFu;
    if (role == R_KV) {
        if (h.h1 < NBLK) { po.o0 = FLG(1, PH_ARDY, h.h1) + lane; if (!hi) po.o1 = FLG(1, PH_QKV, h.h1) + 64 + ix; }
        if (h.h2 < NBLK && hi && ix < ((ti & 8) ? ph_count(PH_FFN2) : ph_count(PH_OUT))) po.o1 = FLG(1, (ti & 8) ? PH_FFN2 : PH_OUT, h.h2) + ix;
        if (h.h0 < NBLK && hi && ix < 4 * SPB) po.o2 = FLG(0, PH_REDB, h.h0) + ix;
        if (h.h0 < NBLK && !hi && ix < ph_count(PH_FFN2)) po.o3 = FLG(0, PH_FFN2, h.h0) + ix;
    } else if (role == R_Q) {
        if (h.h0 < NBLK) { po.o0 = FLG(0, PH_ARDY, h.h0) + lane; if (!hi) po.o1 = FLG(0, PH_QKV, h.h0) + 64 + ix; if (lane == 0) po.o3 = FLG(0, PH_ARDY, h.h0) + 64; }
        if (h.h1 < NBLK && hi) { if (ix < 4 * SPB) po.o1 = FLG(0, PH_REDB, h.h1) + ix; else if (ix == 4 * SPB && h.h1 + QHOLD < NBLK) po.o1 = FLG(0, PH_ARDY, h.h1 + QHOLD); }
    } else {
        const int pa = role == R_OUT ? PH_ATT : role == R_FFN1 ? PH_REDA : role == R_FFN2 ? PH_FFN1 : PH_FFN2, pb = role == R_RED ? PH_OUT : pa;
        if (h.h0 < NBLK && !hi && ix < ph_count(pa)) po.o1 = FLG(0, pa, h.h0) + ix;
        if (TBL && role != R_RED && h.h1 + 1 < NBLK && lane < ph_count(pb)) po.o0 = FLG(1, pb, h.h1 + 1) + lane;
        if (XHOLD && role != R_RED && h.h0 < NBLK) { const int pp = role == R_OUT ? PH_ARDY : role == R_FFN1 ? PH_OUT : PH_REDA; if (lane < ph_count(pp)) po.o0 = FLG(0, pp, h.h0) + lane; }
        if (h.h1 < NBLK && hi && ix < ph_count(pb)) po.o1 = FLG(role == R_RED ? 0 : 1, pb, h.h1) + ix;
        if (h.h2 < NBLK && !hi) po.o2 = FLG(0, role == R_OUT ? PH_OUT : PH_FFN2, h.h2) + ix;
    }
}
__device__ __forceinline__ int ws_pick(int role, const Heads& h, unsigned m) {
    if (role == R_KV) {
        if (h.h0 < NBLK && (m & 16u)) return 0;
        if (KVHOLD && h.h0 < NBLK && (m & 32u)) return -1;
        if (h.h1 < NBLK && (m & 3u) == 3u && (m & 128u)) return 1;
        if (h.h2 < NBLK && (m & 4u) && (m & 256u)) return 2;
        if (!(m & 512u)) return -1;
    } else if (role == R_Q) {
        if (h.h0 < NBLK && (m & 3u) == 3u) return 0;
        if (ATHOLD && h.h0 < NBLK && h.h4 > h.h0 && (m & 32u)) return -1;
        if (h.h1 < NBLK && (m & 4u)) return 1;
    } else {
        if (h.h2 < NBLK && h.h0 > h.h2) return (m & 8u) ? 2 : -1;
        if (h.h0 < NBLK && (m & 2u)) return 0;
        if (((XHOLD >> (role - R_OUT)) & 1) && h.h0 < NBLK && (m & 1u)) return -1;
        if (h.h1 < NBLK && (m & 4u) && ((m & 512u) || (TBL && (m & 1u) && h.h1 + 1 < NBLK))) return 1;
    }
    return h.h4 < NBLK ? 4 : -1;
}
__device__ __forceinline__ int ws_wait(LAS unsigned* sw, int role, int ti, const Heads& h, const unsigned* ctr, unsigned* tmo, unsigned tdue) {
    int tid = threadIdx.x; asm volatile("" : "+v"(tid));
    if (tid < 64) {
        PollOff po; ws_poll_offsets(role, ti, h, tid, po);
        unsigned spins = 0; int st;
        for (;;) {
            const unsigned v0 = ws_poll_ld(ctr, po.o0), v1 = ws_poll_ld(ctr, po.o1), v2 = ws_poll_ld(ctr, po.o2), v3 = ws_poll_ld(ctr, po.o3);
            st = ws_pick(role, h, ws_poll_mask(v0, v1, v2, v3) | ws_time_mask(tdue));
            if (st >= 0) break;
            __builtin_amdgcn_s_sleep(PSLEEP);
            if (++spins > 8u * WK_CAP) { st4_sc1(tmo, 9u); break; }
            if ((spins & 63u) == 0u && __builtin_amdgcn_readfirstlane(__hip_atomic_load((GAS unsigned*)tmo, __ATOMIC_RELAXED, __HIP_MEMORY_SCOPE_AGENT)) != 0u) break;
        }
        *sw = (unsigned)st;
    }
    __syncthreads();
    const int st = (int)__builtin_amdgcn_readfirstlane(*sw);
    __syncthreads();
    return st;
}
__device__ __forceinline__ void ln_start(int wk) {
    int tid = threadIdx.x; asm volatile("" : "+v"(tid));
    const int wid = __builtin_amdgcn_readfirstlane(tid >> 6), lane = tid & 63;
    KArgP ap = (KArgP)__builtin_amdgcn_kernarg_segment_ptr();
    __amdgpu_buffer_rsrc_t rs = __builtin_amdgcn_make_buffer_rsrc((void*)ap->ws, 0, 0x10000000, 0x00020000);
    const int r0 = (wk - 96) * 8 + wid;
    u32x4 xr[3][2];
#pragma unroll
    for (int i = 0; i < 3; ++i) { const int row = r0 + 768 * i < S_ ? r0 + 768 * i : r0;
        xr[i][0] = *(const u32x4*)(ap->x + (size_t)row * DM + lane * 8); xr[i][1] = *(const u32x4*)(ap->x + (size_t)row * DM + 512 + lane * 8); }
#pragma unroll
    for (int i = 0; i < 3; ++i) if (r0 + 768 * i < S_) {
        float v[16];
        unpack8(xr[i][0], v); unpack8(xr[i][1], v + 8);
        ln16(v, rs, ap->o_xn1 + (unsigned)(r0 + 768 * i) * 2048u, lane, false);
    }
    wait_vm<0>();
    WS_BAR();
    if (tid == 0) st4_sc1(ap->ch.ctr + (FLG_LN + (wk - 96)) * FSTR, 1u);
}
__device__ __forceinline__ void red_role(LAS unsigned char* lds, int wk) {
    const int tid = threadIdx.x, wid = __builtin_amdgcn_readfirstlane(tid >> 6), ti = wk - 188;
    KArgP ap = (KArgP)__builtin_amdgcn_kernarg_segment_ptr();
    const __attribute__((address_space(4))) MegaArgs& a = *ap;
    __amdgpu_buffer_rsrc_t rs = __builtin_amdgcn_make_buffer_rsrc((void*)a.ws, 0, 0x10000000, 0x00020000);
    unsigned* ctr = a.ch.ctr;
    Heads h = {0, 0, NBLK, NBLK};
    int seq = 64; (void)seq;
    if (MERGE_LN && !HELP) ln_start(wk);
#pragma unroll 1
    while (h.h0 < NBLK) {
        const int sidx_ = wk * 256 + seq; (void)sidx_; ++seq;
        WSTAMP(0);
        const int st = ws_wait((LAS unsigned*)lds, R_RED, ti, h, ctr, a.ch.tmo, 0u);
        if (st < 0) return;
        const bool ra = st == 1;
        const int b = ra ? h.h1++ : h.h0++, ph = ra ? PH_REDA : PH_REDB;
        WSTAMP(1);
        red_task<1 + RED2>(a.o_slab, ra ? nullptr : a.b2, ra ? a.x : nullptr, ra ? a.o_xb0 : a.o_xa0, nullptr, rs, ra ? a.o_xa0 : a.o_xb0, ra ? a.o_xna0 : a.o_xnb0, b * MB + (ti * 8 + wid) * (1 + RED2));
        WSTAMP(2);
        wait_vm<0>();
        WS_BAR();
        WSTAMP(3);
        if (tid == 0) { st4_sc1(ctr + (FLG(0, ph, b) + ti) * FSTR, 1u); if (RED2) st4_sc1(ctr + (FLG(0, ph, b) + 4 + ti) * FSTR, 1u); }
    }
}
#define HQ0 6
#define HNA 5
#define HNB 7
#define HNC 8
#define WDH 8
__device__ __forceinline__ void worker_role(LAS unsigned char* lds, int wk) {
    const int tid = threadIdx.x, wid = __builtin_amdgcn_readfirstlane(tid >> 6);
#pragma unroll 1
  for (int pass = (HELP && wk >= 96) ? 0 : 1; pass < (wk < 188 ? 2 : 1); ++pass) {
    const int role = pass == 0 ? R_Q : wk < 64 ? R_KV : wk < 96 ? R_Q : wk < 128 ? R_OUT : wk < 156 ? R_FFN1 : R_FFN2;
    const int ti = pass == 0 ? 64 + ((wk - 96) & 31) : wk - (role <= R_Q ? 0 : role == R_OUT ? 96 : role == R_FFN1 ? 128 : 156);
    const int hg = wk < 128 ? 0 : wk >= 156 && wk < 188 ? 2 : 1;
    const int s4 = pass == 0 ? HQ0 + (hg > 0 ? HNA : 0) + (hg > 1 ? HNB : 0) : 0;
    const int e4 = pass == 0 ? s4 + (hg == 0 ? HNA : hg == 1 ? HNB : HNC) : (HELP && role == R_Q) ? HQ0 : NBLK;
    const int s4b = (pass == 0 || role != R_Q || !HELP) ? NBLK : HQ0 + HNA + HNB + HNC;
    const int ks = ti & 3, ct = ti >> 2;
    bf16x8 bw[16][2];
    {
        KArgP ap = (KArgP)__builtin_amdgcn_kernarg_segment_ptr();
        const bf16_t* W; int ldb;
        if (role == R_KV) { W = ((ti & 1) ? ap->ch.Wv : ap->ch.Wk) + (ti >> 1) * 128; ldb = 4096; }
        else if (role == R_Q) { W = ap->ch.Wq + (ti - 64) * 128; ldb = 4096; }
        else if (role == R_OUT) { W = ap->ch.Wo + (size_t)ks * 1024 * 1024 + ct * 128; ldb = 1024; }
        else if (role == R_FFN1) { W = ap->w1 + ti * 128; ldb = FF; }
        else { W = ap->w2 + (size_t)ks * 896 * 1024 + ct * 128; ldb = 1024; }
        if (MERGE_FILL && role == R_KV) {
            __amdgpu_buffer_rsrc_t rs = __builtin_amdgcn_make_buffer_rsrc((void*)ap->ws, 0, 0x10000000, 0x00020000);
            const u32x4 sv4 = {SENT, SENT, SENT, SENT};
            const unsigned o_fill = ap->o_fill, n_fill = ap->n_fill;
            for (unsigned i = (unsigned)(ti * 512 + tid); i < n_fill; i += 32768u) st16_sc1(sv4, rs, o_fill + i * 16u);
        } else if (MERGE_LN && wk >= 96 && pass == (HELP ? 0 : 1)) ln_start(wk);
        { const int dl = pass == 0 ? WDH : role == R_KV ? 0 : role == R_Q ? WDQ : HELP ? 0 : WDO; for (int k = 0; k < dl; ++k) __builtin_amdgcn_s_sleep(32); }
        ws_load_weights(lds, W, ldb, 512u * ldb, role == R_FFN2 ? 7 : 8, bw);
    }
    LAS unsigned* sw = (LAS unsigned*)lds;
    if (MERGE_LN && role <= R_Q) {
        if (tid < 64) {
            KArgP ap = (KArgP)__builtin_amdgcn_kernarg_segment_ptr();
            const unsigned* ctr = ap->ch.ctr; unsigned spins = 0;
            for (;;) {
                const unsigned v0 = ws_poll_ld(ctr, FLG_LN + tid), v1 = ws_poll_ld(ctr, tid < 32 ? FLG_LN + 64 + tid : 0xFFFFFFFFu);
                if (__all(v0 != 0u && v1 != 0u)) break;
                __builtin_amdgcn_s_sleep(PSLEEP);
                if (++spins > 8u * WK_CAP) { st4_sc1(ap->ch.tmo, 9u); break; }
            }
        }
        __syncthreads();
    }
    Heads h;
    if (role == R_KV) { h.h0 = 0; h.h1 = ti >> 4; h.h2 = ((ti >> 4) + ((ti & 8) ? 3 : 2)) & 3; h.h4 = 0; }
    else if (pass == 0) { h.h0 = NBLK; h.h1 = NBLK; h.h2 = NBLK; h.h4 = s4; }
    else if (role == R_Q) { h.h0 = (ti - 64) >> 4; h.h1 = 0; h.h2 = NBLK; h.h4 = 0; }
    else { h.h0 = 0; h.h1 = 0; h.h2 = (role == R_FFN1 || RED2) ? NBLK : ct; h.h4 = NBLK; }
    int seq = (HELP && wk >= 96 && pass == 1) ? 64 : 0; (void)seq;
    int st = role <= R_Q ? 4 : -1;
    unsigned pre = 0u;
    unsigned tdue = 0u;
#pragma unroll 1
    while (h.h0 < NBLK || h.h1 < NBLK || h.h2 < NBLK || h.h4 < NBLK) {
        KArgP ap = (KArgP)__builtin_amdgcn_kernarg_segment_ptr(); asm volatile("" : "+s"(ap));
        const __attribute__((address_space(4))) MegaArgs& a = *ap;
        __amdgpu_buffer_rsrc_t rs = __builtin_amdgcn_make_buffer_rsrc((void*)a.ws, 0, 0x10000000, 0x00020000);
        unsigned* ctr = a.ch.ctr;
        const int sidx_ = wk * 256 + seq; (void)sidx_; ++seq;
        WSTAMP(0);
        if (st < 0) { st = ws_wait(sw, role, ti, h, ctr, a.ch.tmo, tdue); if (st < 0) return; }
        if (TWIN && st == 0) tdue = (h.h0 + 1 < NBLK && role != R_Q && ((TWIN >> (role == R_KV ? 0 : role - 1)) & 1)) ? (ws_now() + PCT) | 1u : 0u;
#define WS_OP(st_, kind_, ph_, l_, b_) do { \
            if (role == R_KV) { kind_ = (st_) == 1 ? K_ATT : (st_) == 2 ? K_RED : K_GEMM; ph_ = (st_) == 1 ? PH_ATT : (st_) == 2 ? ((ti & 8) ? PH_REDB : PH_REDA) : PH_QKV; l_ = (st_) == 4 ? 0 : 1; } \
            else if (role == R_Q) { kind_ = (st_) == 0 ? K_ATT : K_GEMM; ph_ = (st_) == 0 ? PH_ATT : PH_QKV; l_ = (st_) == 1 ? 1 : 0; } \
            else if ((st_) == 2) { kind_ = K_RED; ph_ = role == R_OUT ? PH_REDA : PH_REDB; l_ = 0; } \
            else { kind_ = K_GEMM; ph_ = role == R_OUT ? PH_OUT : role == R_FFN1 ? PH_FFN1 : PH_FFN2; l_ = (st_); } \
            b_ = (st_) == 0 ? h.h0 : (st_) == 1 ? h.h1 : (st_) == 2 ? h.h2 : h.h4; } while (0)
#define WS_AOP(l_, b_, A_, lda_) do { const char* _ws = a.ws; \
            if (role <= R_Q) { A_ = (const bf16_t*)(_ws + ((l_) ? a.o_xnb0 : a.o_xn1)) + (size_t)(b_) * MB * 1024; lda_ = 1024; } \
            else if (role == R_OUT) { A_ = (const bf16_t*)(_ws + a.o_ob) + ((size_t)(l_) * S_ + (b_) * MB) * 4096 + ks * 1024; lda_ = 4096; } \
            else if (role == R_FFN1) { A_ = (const bf16_t*)(_ws + ((l_) ? a.o_xna1 : a.o_xna0)) + (size_t)(b_) * MB * 1024; lda_ = 1024; } \
            else { A_ = (const bf16_t*)(_ws + a.o_hb) + ((size_t)(l_) * S_ + (b_) * MB) * FF + ks * 896; lda_ = FF; } } while (0)
        int kind, ph, l, b;
        WS_OP(st, kind, ph, l, b);
        {
            const int inc = role == R_KV ? ((st == 1 || st == 2) ? 4 : 1) : role == R_Q ? (st == 0 ? 2 : 1) : st == 2 ? 8 : 1;
            if (st == 0) h.h0 += inc; else if (st == 1) h.h1 += inc; else if (st == 2) h.h2 += inc; else { h.h4 += inc; if (h.h4 == e4) h.h4 = s4b; }
        }
        WSTAMP(1);
        int sub = ti;
        if (kind == K_GEMM) {
            const bf16_t* A; int lda;
            WS_AOP(l, b, A, lda);
            if (!pre) ws_issue(lds, A, lda, role == R_FFN2 ? 7 : 8);
            auto pollf = [&](int ln, PollOff& po) { ws_poll_offsets(role, ti, h, ln, po); };
            int ldc, mode; unsigned co; const bf16_t* bias = nullptr;
            if (role <= R_Q) { mode = 0; ldc = 12288; co = a.o_qkv + (unsigned)l * QSTRIDE + ((unsigned)b * MB * 12288u + (role == R_Q ? (ti - 64) * 384u : (ti >> 1) * 384u + 128u + (ti & 1) * 128u)) * 2u; }
            else if (role == R_FFN1) { mode = 1; ldc = FF; co = a.o_hb + (((unsigned)l * S_ + b * MB) * FF + ti * 128u) * 2u; bias = a.b1 + ti * 128; }
            else { mode = 2; ldc = 1024; co = a.o_slab + ((unsigned)ks * (S_ * DM) + (unsigned)b * MB * 1024u + ct * 128u) * 4u; }
            {
                f32x4 res[1][RG / 2][2];
                unsigned m;
                unsigned abb = 0u, abl = 0u; bool can = false;
                if (ABRT && st != 0 && h.h0 < NBLK && ((ABRT >> (role == R_KV ? 1 : role == R_Q ? 2 : 3)) & 1)) {
                    can = true;
                    const int pa = role == R_KV ? PH_REDB : role == R_Q ? PH_ARDY : role == R_OUT ? PH_ATT : role == R_FFN1 ? PH_REDA : PH_FFN1;
                    abb = FLG(0, pa, h.h0); abl = ph_count(pa) - 1;
                }
                if (role == R_FFN2) m = ws_compute<7>(lds, A, lda, bw, res, ctr, pollf, tdue, abb, abl, can); else m = ws_compute<8>(lds, A, lda, bw, res, ctr, pollf, tdue, abb, abl, can);
                if (ABRT && (m & 0x80000000u)) {
                    if (st == 1) h.h1 = b; else h.h4 = b;
                    wait_vm<0>(); WS_BAR();
                    st = role == R_Q ? -1 : 0; pre = 0u;
                    continue;
                }
                st = ws_pick(role, h, m);
                ws_store(res[0], mode, rs, co, ldc, bias);
            }
            pre = 0u;
            WSTAMP(2);
            if (st >= 0) {
                int k2, p2, l2, b2;
                WS_OP(st, k2, p2, l2, b2); (void)p2;
                if (k2 == K_GEMM) { pre = 1u; WS_AOP(l2, b2, A, lda); ws_issue(lds, A, lda, role == R_FFN2 ? 7 : 8); }
            }
            if (!pre) wait_vm<0>(); else if (NUP == 8 && role == R_FFN2) wait_vm<7 * (RG / 2)>(); else wait_vm<NUP * (RG / 2)>();
        } else {
            st = -1; pre = 0u;
            if (kind == K_ATT) {
                sub = ti & 15;
                const int task = sub * 8 + wid, seg = b * SPB + (task >> 6), hh = (task >> 1) & 31;
                attn_task(lds, seg, hh, task & 1, a.o_qkv + (unsigned)l * QSTRIDE, a.o_kv + (unsigned)(l * 2) * KVR * 32 * 2048, a.o_kv + (unsigned)(l * 2 + 1) * KVR * 32 * 2048, rs, a.o_ob + (unsigned)l * (S_ * 4096u * 2u), a.ws);
            } else {
                sub = l ? ti & 7 : 4 + (ti & 3);
                const bool ra = ph == PH_REDA;
                red_task<1>(a.o_slab, ra ? nullptr : a.b2, (!l && ra) ? a.x : nullptr, l ? (ra ? a.o_xb0 : a.o_xa1) : a.o_xa0, (l && !ra) ? a.out : nullptr, rs,
                            l ? a.o_xa1 : (ra ? a.o_xa0 : a.o_xb0), l ? a.o_xna1 : (ra ? a.o_xna0 : a.o_xnb0), b * MB + sub * 8 + wid);
            }
            WSTAMP(2);
            wait_vm<0>();
        }
        WS_BAR();
        WSTAMP(3);
        if (wid == 0) { if ((tid & 63) == 0) st4_sc1(ctr + (FLG(l, ph, b) + sub) * FSTR, 1u); }
    }
  }
}

__global__ __launch_bounds__(512, 2) void mega_kernel(MegaArgs a) {
    extern __shared__ __attribute__((aligned(1024))) unsigned char shm[];
    LAS unsigned char* lds = (LAS unsigned char*)shm;
#define CROT 0
    if (blockIdx.x < 64) chain_role(a.ch, lds, ((blockIdx.x & 7) << 3) | (((blockIdx.x >> 3) + CROT) & 7));
    else {
        if (HELP || blockIdx.x < 252) worker_role(lds, (int)blockIdx.x - 64);
        if (blockIdx.x >= 252) red_role(lds, (int)blockIdx.x - 64);
    }
}

#define LAG 14
extern "C" void kernel_launch(void* const* d_in, const int* in_sizes, int n_in, void* d_out, int out_size, void* d_ws, size_t ws_size, hipStream_t stream) {
    const bf16_t* x = (const bf16_t*)d_in[0]; const bf16_t* mem0 = (const bf16_t*)d_in[1];
    const bf16_t* Wq = (const bf16_t*)d_in[2]; const bf16_t* Wk = (const bf16_t*)d_in[3]; const bf16_t* Wv = (const bf16_t*)d_in[4]; const bf16_t* Wo = (const bf16_t*)d_in[5];
    const bf16_t* w1 = (const bf16_t*)d_in[8]; const bf16_t* b1 = (const bf16_t*)d_in[9]; const bf16_t* w2 = (const bf16_t*)d_in[10]; const bf16_t* b2 = (const bf16_t*)d_in[11];
    char* ws = (char*)d_ws; size_t off = 0;
    auto alloc = [&](size_t bytes) { size_t p = off; off += (bytes + 255) & ~(size_t)255; return p; };
    const int T = 64 + LAG;
    const size_t o_ctr = alloc((size_t)FLG_SLOTS * FSTR * 4), o_tmo = alloc(256), o_zero = alloc(8192 + 64 * 256);
    const size_t zero_bytes = off;
    const size_t o_mem = alloc((size_t)(T + 1) * NCOPY * 32768), o_U = alloc(64 * 32768), o_X = (alloc(64 * XSTR + 2 * XSTR) + 2 * XSTR - 1) & ~(size_t)(2 * XSTR - 1);
    const size_t o_kv = alloc((size_t)4 * KVR * 32 * 1024 * 2);
    const size_t XB = (size_t)S_ * DM * 2;
    const size_t o_xn1 = alloc(XB), o_xa0 = alloc(XB), o_xna0 = alloc(XB), o_xb0 = alloc(XB), o_xnb0 = alloc(XB), o_xna1 = alloc(XB), o_xa1 = alloc(XB);
    const size_t o_qkv = alloc((size_t)2 * QSTRIDE), o_ob = alloc((size_t)2 * S_ * 4096 * 2), o_hb = alloc((size_t)2 * S_ * FF * 2);
    const size_t o_slab = alloc((size_t)4 * S_ * DM * 4);

    hipMemsetAsync(ws, 0, zero_bytes, stream);
    MegaArgs a;
    a.ch.mem0 = mem0; a.ch.Wq = Wq; a.ch.Wk = Wk; a.ch.Wv = Wv; a.ch.Wo = Wo; a.ch.wsb = ws; a.ch.o_qkv = o_qkv; a.ch.o_mem = o_mem; a.ch.o_U = o_U; a.ch.o_X = o_X; a.ch.o_kv = o_kv; a.ch.o_zero = o_zero;
    a.ch.tmo = (unsigned*)(ws + o_tmo); a.ch.ctr = (unsigned*)(ws + o_ctr); a.ch.lag = LAG; a.ch.nstep = T; a.ch.qstride = QSTRIDE; a.ch.pad0 = 0;
    a.ws = ws; a.x = x; a.b1 = b1; a.b2 = b2; a.out = (bf16_t*)d_out;
    a.w1 = w1; a.w2 = w2; a.o_xn1 = o_xn1; a.o_xa0 = o_xa0; a.o_xna0 = o_xna0; a.o_xb0 = o_xb0; a.o_xnb0 = o_xnb0; a.o_xna1 = o_xna1;
    a.o_qkv = o_qkv; a.o_ob = o_ob; a.o_hb = o_hb; a.o_slab = o_slab; a.o_kv = o_kv; a.o_xa1 = o_xa1;
    a.o_fill = (unsigned)(o_mem + NCOPY * 32768); a.n_fill = (unsigned)((o_kv - o_mem - NCOPY * 32768) / 16);

    hipFuncSetAttribute((const void*)mega_kernel, hipFuncAttributeMaxDynamicSharedMemorySize, CH_LDS);
    mega_kernel<<<256, 512, CH_LDS, stream>>>(a);
}
```
